```python
import torch

def _softmax(x, axis=-1):
    return torch.softmax(x, dim=axis)
def _gelu(x):
    return torch.nn.functional.gelu(x, approximate="tanh")
def _silu(x):
    return torch.nn.functional.silu(x)
def _sigmoid(x):
    return torch.sigmoid(x)
def _layernorm(x, axis=-1, eps=1e-5):
    m = x.mean(dim=axis, keepdim=True)
    v = x.var(dim=axis, keepdim=True, unbiased=False)
    return (x - m) / torch.sqrt(v + eps)
def _scatter_add(tgt, idx, src, axis=0):
    return tgt.clone().index_add_(axis, idx, src)
def _segment_sum(src, seg, n):
    out = torch.zeros((n,) + src.shape[1:], dtype=src.dtype)
    return out.index_add_(0, seg, src)
def _pad(x, pw):
    flat = tuple(v for pair in reversed(pw) for v in pair)
    return torch.nn.functional.pad(x, flat)

kernel_name = 'interleave_layers__sliding_block_cached__mixer_token_mlp__decode'

def setup_inputs():

    _w = (lambda a: (lambda t: t.to(torch.bfloat16) if t.is_floating_point() else t)(torch.as_tensor(a)))
    def _A_setup():
        B, Sq, D = 4, 1, 512
        Hq, Hkv, Sk, Dh = 32, 8, 16384, 128
        assert Hq % Hkv == 0, (Hq, Hkv)
        return {
            "x": _w((lambda *s: torch.randn(*s, dtype=torch.bfloat16) * 0.05)(B, Sq, D)),
            "k_cache": _w((lambda *s: torch.randn(*s, dtype=torch.bfloat16) * 0.8)(B, Hkv, Sk, Dh)),
            "v_cache": _w((lambda *s: torch.randn(*s, dtype=torch.bfloat16) * 1.6)(B, Hkv, Sk, Dh)),
            "wq": _w((lambda *s: torch.randn(*s, dtype=torch.bfloat16) * 0.8)(D, Hq * Dh) * (D ** -0.5)),
            "wo": _w((lambda *s: torch.randn(*s, dtype=torch.bfloat16) * 1.6)(Hq * Dh, D) * ((Hq * Dh) ** -0.5)),
        }
    _A_in = _A_setup()

    def _B_setup():
        B, S, D, F = 4, 1, 512, 1792
        return {
            "x": _w((lambda *s: torch.randn(*s, dtype=torch.bfloat16) * 0.05)(B, S, D)),
            "w1": _w((lambda *s: torch.randn(*s, dtype=torch.bfloat16) * 0.8)(S, F) * (S ** -0.5)),
            "b1": _w((lambda *s: torch.randn(*s, dtype=torch.bfloat16) * 0.2)(F) * 0.02),
            "w2": _w((lambda *s: torch.randn(*s, dtype=torch.bfloat16) * 0.2)(F, S) * (F ** -0.5)),
            "b2": _w((lambda *s: torch.randn(*s, dtype=torch.bfloat16) * 0.2)(S) * 0.02),
        }
    _B_in = _B_setup()

    return {
        "A_x": _A_in["x"],
        "A_k_cache": _A_in["k_cache"],
        "A_v_cache": _A_in["v_cache"],
        "A_wq": _A_in["wq"],
        "A_wo": _A_in["wo"],
        "B_x": _B_in["x"],
        "B_w1": _B_in["w1"],
        "B_b1": _B_in["b1"],
        "B_w2": _B_in["w2"],
        "B_b2": _B_in["b2"],
    }

def reference(A_x, A_k_cache, A_v_cache, A_wq, A_wo, B_x, B_w1, B_b1, B_w2, B_b2):
    def _A_ref(x, k_cache, v_cache, wq, wo):
        B, Sq, D = x.shape
        Hq, Dh = 32, 128
        Hkv, Sk = k_cache.shape[1], k_cache.shape[2]
        W = min(4096, max(256, Sk // 4))
        rep = Hq // Hkv
        q = (lambda x, *d: x.permute(*d))((x @ wq).reshape(B, Sq, Hq, Dh), 0, 2, 1, 3)
        k = (lambda x, n, axis: torch.repeat_interleave(x, n, dim=axis))(k_cache[:, :, Sk - W:, :], rep, axis=1)
        v = (lambda x, n, axis: torch.repeat_interleave(x, n, dim=axis))(v_cache[:, :, Sk - W:, :], rep, axis=1)
        scores = torch.einsum("bhqd,bhkd->bhqk", q, k) * (Dh ** -0.5)
        p = _softmax(scores, axis=-1)
        o = torch.einsum("bhqk,bhkd->bhqd", p, v)
        return (lambda x, *d: x.permute(*d))(o, 0, 2, 1, 3).reshape(B, Sq, Hq * Dh) @ wo

    def _B_ref(x, w1, b1, w2, b2):
        B, S, D = x.shape
        h = _layernorm(x, axis=-1)
        u = torch.einsum("bsd,sf->bdf", h, w1) + b1
        u = _gelu(u)
        y = torch.einsum("bdf,fs->bds", u, w2) + b2
        return (lambda x, *d: x.permute(*d))(y, 0, 2, 1)

    x = A_x
    x = x + _A_ref(_layernorm(x, axis=-1), A_k_cache, A_v_cache, A_wq, A_wo)
    x = x + _B_ref(_layernorm(x, axis=-1), B_w1, B_b1, B_w2, B_b2)
    x = x + _A_ref(_layernorm(x, axis=-1), A_k_cache, A_v_cache, A_wq, A_wo)
    x = x + _B_ref(_layernorm(x, axis=-1), B_w1, B_b1, B_w2, B_b2)
    return x
```

```cpp
#include <hip/hip_runtime.h>
#include <hip/hip_bf16.h>
#include <stdint.h>

typedef unsigned short u16;
__device__ __forceinline__ float bf2f(u16 b) { return __uint_as_float(((unsigned)b) << 16); }
__device__ __forceinline__ u16 f2bf(float f) { unsigned u = __float_as_uint(f); u += 0x7FFFu + ((u >> 16) & 1u); return (u16)(u >> 16); }

constexpr int B = 4, D = 512, HQ = 32, HKV = 8, SK = 16384, DH = 128, W = 4096, F = 1792;
constexpr int NS = 8;

__device__ float block_sum(float v, float* sh) {
    for (int m = 32; m >= 1; m >>= 1) v += __shfl_xor(v, m);
    __syncthreads();
    if ((threadIdx.x & 63) == 0) sh[threadIdx.x >> 6] = v;
    __syncthreads();
    float s = 0; for (int i = 0; i < (int)(blockDim.x >> 6); ++i) s += sh[i];
    return s;
}

__global__ void k_init(const u16* x, float* xf) { int i = blockIdx.x * 256 + threadIdx.x; xf[i] = bf2f(x[i]); }

__global__ void k_q(const float* xf, const u16* wq, float* q) {
    __shared__ float h[D]; __shared__ float sh[8];
    int b = blockIdx.y, t = threadIdx.x;
    float a0 = xf[b * D + t], a1 = xf[b * D + 256 + t];
    float m = block_sum(a0 + a1, sh) / D;
    float v = block_sum((a0 - m) * (a0 - m) + (a1 - m) * (a1 - m), sh) / D;
    float r = rsqrtf(v + 1e-5f);
    h[t] = (a0 - m) * r; h[256 + t] = (a1 - m) * r;
    __syncthreads();
    int col = blockIdx.x * 256 + t;
    float acc = 0;
    for (int k = 0; k < D; ++k) acc += h[k] * bf2f(wq[(size_t)k * 4096 + col]);
    q[b * 4096 + col] = acc;
}

__global__ void k_attn(const float* q, const u16* kc, const u16* vc, float* po, float* pml) {
    __shared__ float qs[4][DH]; __shared__ float sc[4][512]; __shared__ float sh[8]; __shared__ float mx[4];
    int s = blockIdx.x, g = blockIdx.y, b = blockIdx.z, t = threadIdx.x;
    for (int i = t; i < 512; i += 256) qs[i >> 7][i & 127] = q[b * 4096 + g * 512 + i];
    __syncthreads();
    size_t base = ((size_t)(b * HKV + g) * SK + (SK - W) + s * 512) * DH;
    for (int key = t; key < 512; key += 256) {
        const u16* kr = kc + base + (size_t)key * DH;
        float a[4] = {0, 0, 0, 0};
        for (int d = 0; d < DH; ++d) { float kv = bf2f(kr[d]); for (int h = 0; h < 4; ++h) a[h] += kv * qs[h][d]; }
        for (int h = 0; h < 4; ++h) sc[h][key] = a[h] * 0.08838834764831845f;
    }
    __syncthreads();
    for (int h = 0; h < 4; ++h) {
        float m = fmaxf(sc[h][t], sc[h][t + 256]);
        for (int o = 32; o >= 1; o >>= 1) m = fmaxf(m, __shfl_xor(m, o));
        __syncthreads();
        if ((t & 63) == 0) sh[t >> 6] = m;
        __syncthreads();
        m = fmaxf(fmaxf(sh[0], sh[1]), fmaxf(sh[2], sh[3]));
        float e0 = __expf(sc[h][t] - m), e1 = __expf(sc[h][t + 256] - m);
        __syncthreads();
        sc[h][t] = e0; sc[h][t + 256] = e1;
        float l = block_sum(e0 + e1, sh);
        if (t == 0) { pml[((b * HQ + g * 4 + h) * NS + s) * 2] = m; pml[((b * HQ + g * 4 + h) * NS + s) * 2 + 1] = l; }
    }
    __syncthreads();
    int d = t & 127, hh = t >> 7;
    float a0 = 0, a1 = 0;
    for (int key = 0; key < 512; ++key) { float vv = bf2f(vc[base + (size_t)key * DH + d]); a0 += sc[hh][key] * vv; a1 += sc[hh + 2][key] * vv; }
    po[((size_t)(b * HQ + g * 4 + hh) * NS + s) * DH + d] = a0;
    po[((size_t)(b * HQ + g * 4 + hh + 2) * NS + s) * DH + d] = a1;
}

__global__ void k_comb(const float* po, const float* pml, float* o) {
    int h = blockIdx.x, b = blockIdx.y, d = threadIdx.x;
    float m = -1e30f; for (int s = 0; s < NS; ++s) m = fmaxf(m, pml[((b * HQ + h) * NS + s) * 2]);
    float L = 0, a = 0;
    for (int s = 0; s < NS; ++s) { float f = __expf(pml[((b * HQ + h) * NS + s) * 2] - m); L += f * pml[((b * HQ + h) * NS + s) * 2 + 1]; a += f * po[((size_t)(b * HQ + h) * NS + s) * DH + d]; }
    o[b * 4096 + h * DH + d] = a / L;
}

__global__ void k_wo(const float* o, const u16* wo, float* xf) {
    __shared__ float red[8][64];
    int b = blockIdx.y, c = blockIdx.x * 64 + (threadIdx.x & 63), w = threadIdx.x >> 6;
    float acc = 0;
    for (int k = w * 512; k < w * 512 + 512; ++k) acc += o[b * 4096 + k] * bf2f(wo[(size_t)k * D + c]);
    red[w][threadIdx.x & 63] = acc;
    __syncthreads();
    if (w == 0) { float s = 0; for (int i = 0; i < 8; ++i) s += red[i][threadIdx.x]; xf[b * D + c] += s; }
}

__global__ void k_mlp(const float* xin, float* xout, const u16* w1, const u16* b1, const u16* w2, const u16* b2, u16* out) {
    __shared__ float sh[8]; __shared__ float h[D];
    int b = blockIdx.y, t = threadIdx.x;
    float a0 = xin[b * D + t], a1 = xin[b * D + 256 + t];
    float m = block_sum(a0 + a1, sh) / D;
    float v = block_sum((a0 - m) * (a0 - m) + (a1 - m) * (a1 - m), sh) / D;
    float r = rsqrtf(v + 1e-5f);
    float h0 = (a0 - m) * r, h1 = (a1 - m) * r;
    float m2 = block_sum(h0 + h1, sh) / D;
    float v2 = block_sum((h0 - m2) * (h0 - m2) + (h1 - m2) * (h1 - m2), sh) / D;
    float r2 = rsqrtf(v2 + 1e-5f);
    h[t] = (h0 - m2) * r2; h[256 + t] = (h1 - m2) * r2;
    __syncthreads();
    for (int e = 0; e < 8; ++e) {
        int c = blockIdx.x * 8 + e; float hv = h[c]; float acc = 0;
        for (int f = t; f < F; f += 256) {
            float u = hv * bf2f(w1[f]) + bf2f(b1[f]);
            float z = 0.7978845608028654f * (u + 0.044715f * u * u * u);
            float gl = 0.5f * u * (1.f + tanhf(z));
            acc += gl * bf2f(w2[f]);
        }
        float sres = block_sum(acc, sh);
        if (t == 0) { float y = xin[b * D + c] + sres + bf2f(b2[0]); xout[b * D + c] = y; if (out) out[b * D + c] = f2bf(y); }
    }
}

extern "C" void kernel_launch(void* const* d_in, const int* in_sizes, int n_in, void* d_out, int out_size, void* d_ws, size_t ws_size, hipStream_t stream) {
    const u16* x = (const u16*)d_in[0]; const u16* kc = (const u16*)d_in[1]; const u16* vc = (const u16*)d_in[2];
    const u16* wq = (const u16*)d_in[3]; const u16* wo = (const u16*)d_in[4];
    const u16* w1 = (const u16*)d_in[6]; const u16* b1 = (const u16*)d_in[7]; const u16* w2 = (const u16*)d_in[8]; const u16* b2 = (const u16*)d_in[9];
    float* ws = (float*)d_ws;
    float* xa = ws; float* xb = ws + 2048; float* q = ws + 4096; float* o = q + 4 * 4096; float* pml = o + 4 * 4096; float* po = pml + 4 * 32 * 8 * 2;
    k_init<<<8, 256, 0, stream>>>(x, xa);
    for (int l = 0; l < 2; ++l) {
        k_q<<<dim3(16, B), 256, 0, stream>>>(xa, wq, q);
        k_attn<<<dim3(NS, HKV, B), 256, 0, stream>>>(q, kc, vc, po, pml);
        k_comb<<<dim3(HQ, B), 128, 0, stream>>>(po, pml, o);
        k_wo<<<dim3(8, B), 512, 0, stream>>>(o, wo, xa);
        k_mlp<<<dim3(64, B), 256, 0, stream>>>(xa, xb, w1, b1, w2, b2, l == 1 ? (u16*)d_out : nullptr);
        float* tmp = xa; xa = xb; xb = tmp;
    }
}
```

```cpp
#include <hip/hip_runtime.h>
#include <stdint.h>

#define GAS __attribute__((address_space(1)))
#define LAS __attribute__((address_space(3)))
#define DI __device__ __forceinline__
typedef unsigned v4u __attribute__((ext_vector_type(4)));
typedef unsigned v2u __attribute__((ext_vector_type(2)));
typedef float f32x4 __attribute__((ext_vector_type(4)));
typedef float f32x2 __attribute__((ext_vector_type(2)));
typedef __bf16 bf16x8 __attribute__((ext_vector_type(8)));
typedef __bf16 bf16x2 __attribute__((ext_vector_type(2)));
typedef short s16x4 __attribute__((ext_vector_type(4)));
typedef unsigned long long u64;
typedef unsigned short u16;

#define STAMP 0
#define KV_AUX " nt"
#define DLY_O 3
#define DLY_Y 12
#define DLY_X 12
#define DLY_Q 3

namespace {

constexpr int SK = 16384, WIN0 = 12288, DH = 128;
constexpr unsigned SENT = 0xFFFFFFFFu;
constexpr unsigned TAGSTEP = 0x9E3779B9u;
constexpr unsigned SPIN_MAX = 1u << 17;
constexpr float EPS = 1e-5f;

constexpr int OFF_E = 0, OFF_READY = 4096, OFF_Q1P = 0x10000, OFF_O = 0x100000, OFF_ML = 0x180000, OFF_Y = 0x190000, OFF_X2 = 0x1B0000,
              OFF_Q2 = 0x1C0000, OFF_STAMP = 0x200000, WS_BYTES = 0x400000;
constexpr int O_L = 0x40000, ML_L = 0x2000, Y_L = 0x10000;
constexpr int L_KW = 0, L_RED = 131072, L_CST = 147456, L_QF = 155648, L_XROW = 156672, L_X2 = 158720, L_ML = 159744, L_ST = 160000,
              L_MRED = 160256, L_FLAG = 160512, L_GW = 160640, L_GQ = L_CST, LDS_BYTES = 162688;

DI float bf2f(unsigned h) { return __builtin_bit_cast(float, h << 16); }
DI unsigned cvtpk(float lo, float hi) { f32x2 v = {lo, hi}; return __builtin_bit_cast(unsigned, __builtin_convertvector(v, bf16x2)); }
DI float bfr(float x) { return __builtin_bit_cast(float, cvtpk(x, x) << 16); }
DI unsigned nosent(unsigned x) { return x == SENT ? 0x7FC07FC0u : x; }
DI unsigned fbits(float x) { return __builtin_bit_cast(unsigned, x); }
DI float bitsf(unsigned x) { return __builtin_bit_cast(float, x); }
DI f32x4 mfma32(v4u a, v4u b, f32x4 c) { return __builtin_amdgcn_mfma_f32_16x16x32_bf16(__builtin_bit_cast(bf16x8, a), __builtin_bit_cast(bf16x8, b), c, 0, 0, 0); }
DI f32x4 mfma16(v2u a, v2u b, f32x4 c) { return __builtin_amdgcn_mfma_f32_16x16x16bf16_1k(__builtin_bit_cast(s16x4, a), __builtin_bit_cast(s16x4, b), c, 0, 0, 0); }
template <int CTRL> DI float dpp(float x) { return bitsf(__builtin_amdgcn_mov_dpp((int)fbits(x), CTRL, 0xf, 0xf, true)); }
constexpr int ROR1 = 0x121, ROR2 = 0x122, ROR4 = 0x124, ROR8 = 0x128, XOR1 = 0xB1;
DI float xrow_max(float x) {
    auto s = __builtin_amdgcn_permlane16_swap(fbits(x), fbits(x), false, false);
    x = fmaxf(bitsf(s[0]), bitsf(s[1]));
    auto t = __builtin_amdgcn_permlane32_swap(fbits(x), fbits(x), false, false);
    return fmaxf(bitsf(t[0]), bitsf(t[1]));
}
DI float xrow_sum(float x) {
    auto s = __builtin_amdgcn_permlane16_swap(fbits(x), fbits(x), false, false);
    x = bitsf(s[0]) + bitsf(s[1]);
    auto t = __builtin_amdgcn_permlane32_swap(fbits(x), fbits(x), false, false);
    return bitsf(t[0]) + bitsf(t[1]);
}
DI float row_sum(float x) { x += dpp<ROR8>(x); x += dpp<ROR4>(x); x += dpp<ROR2>(x); x += dpp<ROR1>(x); return x; }
DI float xrow_scatter_sum(float y0, float y1, float y2, float y3) {
    auto a = __builtin_amdgcn_permlane32_swap(fbits(y0), fbits(y2), false, false);
    auto b = __builtin_amdgcn_permlane32_swap(fbits(y1), fbits(y3), false, false);
    const float z0 = bitsf(a[0]) + bitsf(a[1]), z1 = bitsf(b[0]) + bitsf(b[1]);
    auto c = __builtin_amdgcn_permlane16_swap(fbits(z0), fbits(z1), false, false);
    return bitsf(c[0]) + bitsf(c[1]);
}
DI float pin(float v) { asm volatile("" : "+v"(v)); return v; }
DI unsigned pinu(unsigned v) { asm volatile("" : "+v"(v)); return v; }
DI float sel4(int k, float a, float b, float c, float d) { a = pin(a); b = pin(b); c = pin(c); d = pin(d); const float lo = (k & 1) ? b : a, hi = (k & 1) ? d : c; return (k & 2) ? hi : lo; }
DI unsigned sel4u(int k, unsigned a, unsigned b, unsigned c, unsigned d) { return fbits(sel4(k, bitsf(a), bitsf(b), bitsf(c), bitsf(d))); }
DI float ex2(float x) { return __builtin_amdgcn_exp2f(x); }
DI float rcpf(float x) { return __builtin_amdgcn_rcpf(x); }
DI float rsqf(float x) { return __builtin_amdgcn_rsqf(x); }
#define BAR() asm volatile("s_waitcnt lgkmcnt(0)\n\ts_barrier" ::: "memory")
#define MEMFENCE() asm volatile("" ::: "memory")
#define SCHED() __builtin_amdgcn_sched_barrier(0)
typedef __amdgpu_buffer_rsrc_t rsrc_t;
DI rsrc_t mk_rsrc(const void* p, unsigned bytes) { return __builtin_amdgcn_make_buffer_rsrc((void*)p, 0, bytes, 0x00020000); }
DI v4u ld128(rsrc_t r, int off) { return __builtin_amdgcn_raw_buffer_load_b128(r, off, 0, 16); }
DI unsigned ld32(rsrc_t r, int off) { return __builtin_amdgcn_raw_buffer_load_b32(r, off, 0, 16); }
DI void st128(rsrc_t r, int off, v4u v) { __builtin_amdgcn_raw_buffer_store_b128(v, r, off, 0, 16); }
DI void st64(rsrc_t r, int off, v2u v) { __builtin_amdgcn_raw_buffer_store_b64(v, r, off, 0, 16); }
DI bool valid4(v4u v) { return (v.x != SENT) & (v.y != SENT) & (v.z != SENT) & (v.w != SENT); }
DI bool wave_all(bool ok) { return __builtin_amdgcn_ballot_w64(!ok) == 0ull; }
template <typename T> DI T lds_ld(unsigned a) { return *(const LAS T*)(size_t)a; }
template <typename T> DI void lds_st(unsigned a, T v) { *(LAS T*)(size_t)a = v; }
DI v2u lds_tr(unsigned a) { return __builtin_bit_cast(v2u, __builtin_amdgcn_ds_read_tr16_b64_v4i16((LAS s16x4*)(size_t)a)); }
DI u64 rt() { return __builtin_amdgcn_s_memrealtime(); }

DI int wimg_voff(int lane, int odd, int rowstride) {
    const int r = lane >> 3, phi = ((r >> 1) & 1) | (odd << 1);
    return r * rowstride + (((lane & 7) ^ (2 * phi)) << 4);
}
#define DMA_W(m0v, voff, rs, soff) asm volatile("s_mov_b32 m0, %0\n\ts_nop 0\n\tbuffer_load_dwordx4 %1, %2, %3 offen" KV_AUX " lds" ::"s"(m0v), "v"(voff), "s"(rs), "s"(soff) : "memory")

struct WFrag { v4u f[2][4]; };
DI void read_wfrag(unsigned img, int lane, v4u (&f)[4], int i) {
    const int gp = lane >> 4, r = (lane >> 2) & 3, cc = lane & 3;
    const int phi = ((r >> 1) & 1) | ((gp & 1) << 1);
#pragma unroll
    for (int n = 0; n < 4; ++n) {
        const unsigned a = img + (8 * gp + r) * 128 + ((n ^ phi) << 5) + cc * 8 + i * 4096;
        const v2u lo = lds_tr(a), hi = lds_tr(a + 512);
        f[n] = v4u{lo.x, lo.y, hi.x, hi.y};
    }
}

template <int N> DI void sleepn() { if constexpr (N > 0) __builtin_amdgcn_s_sleep(N); }

#define STP(i) do { } while (0)

struct Ctx {
    int c, g, b, s, tid, lane, w, gp, cc;
    rsrc_t ws;
};
DI void refresh(Ctx& x) {
    int l = __builtin_amdgcn_mbcnt_hi(~0u, __builtin_amdgcn_mbcnt_lo(~0u, 0u));
    asm volatile("" : "+v"(l));
    x.lane = l; x.gp = l >> 4; x.cc = l & 15; x.tid = x.w * 64 + l;
}

struct AttnOut { float o[8]; float m, l; };

constexpr float SCL = 0.08838834764831845f * 1.4426950408889634f;

DI void qk_softmax(const v4u (&K)[4][4], const v4u (&q)[4], v4u (&P)[2], float& mo, float& lo) {
    f32x4 S[4];
#pragma unroll
    for (int t = 0; t < 4; ++t) {
        S[t] = f32x4{0.f, 0.f, 0.f, 0.f};
#pragma unroll
        for (int i = 0; i < 4; ++i) S[t] = mfma32(K[t][i], q[i], S[t]);
    }
    float m = fmaxf(fmaxf(S[0][0], S[0][1]), fmaxf(S[0][2], S[0][3]));
#pragma unroll
    for (int t = 1; t < 4; ++t) m = fmaxf(m, fmaxf(fmaxf(S[t][0], S[t][1]), fmaxf(S[t][2], S[t][3])));
    m = xrow_max(m);
    const float ms = m * SCL;
    float l = 0.f;
#pragma unroll
    for (int t = 0; t < 4; ++t)
#pragma unroll
        for (int r = 0; r < 4; ++r) { S[t][r] = ex2(fmaf(S[t][r], SCL, -ms)); l += S[t][r]; }
#pragma unroll
    for (int u = 0; u < 2; ++u)
        P[u] = v4u{cvtpk(S[2 * u][0], S[2 * u][1]), cvtpk(S[2 * u][2], S[2 * u][3]), cvtpk(S[2 * u + 1][0], S[2 * u + 1][1]), cvtpk(S[2 * u + 1][2], S[2 * u + 1][3])};
    mo = ms;
    lo = xrow_sum(l);
}

DI void merge_publish(Ctx& x, int layer, const f32x4 (&acc)[8], float m, float l) {
    refresh(x);
    const unsigned red = L_RED + x.w * 2048 + x.gp * 256 + x.cc * 16;
    lds_st<f32x4>(red, f32x4{acc[0][0], acc[1][0], acc[2][0], acc[3][0]});
    lds_st<f32x4>(red + 1024, f32x4{acc[4][0], acc[5][0], acc[6][0], acc[7][0]});
    if ((x.cc & 3) == 0 && (x.cc >> 2) == x.gp) lds_st<f32x2>(L_ML + (x.gp * 8 + x.w) * 8, f32x2{m, l});
    BAR();
    const int h = x.w >> 1, ch = 8 * (x.w & 1) + (x.cc >> 1), hf = x.cc & 1;
    const f32x4 ml = lds_ld<f32x4>(L_ML + (h * 8 + 2 * x.gp) * 8);
    const unsigned ra = L_RED + (2 * x.gp) * 2048 + hf * 1024 + h * 256 + ch * 16;
    const f32x4 oa = lds_ld<f32x4>(ra), ob = lds_ld<f32x4>(ra + 2048);
    const float M = xrow_max(fmaxf(ml[0], ml[2]));
    const float fa = ex2(ml[0] - M), fb = ex2(ml[2] - M);
    const float L = xrow_sum(fa * ml[1] + fb * ml[3]);
    float o[4];
#pragma unroll
    for (int q = 0; q < 4; ++q) o[q] = xrow_sum(fa * oa[q] + fb * ob[q]);
    const unsigned p0 = nosent(cvtpk(o[0], o[1])), p1 = nosent(cvtpk(o[2], o[3]));
    const unsigned n0 = fbits(dpp<XOR1>(bitsf(p0))), n1 = fbits(dpp<XOR1>(bitsf(p1)));
    const int slot = (x.b * 8 + x.g) * 8 + x.s;
    if (x.lane < 16 && hf == 0) st128(x.ws, OFF_O + layer * O_L + slot * 1024 + h * 256 + ch * 16, v4u{p0, p1, n0, n1});
    if (x.lane == 1 && (x.w & 1) == 0) st64(x.ws, OFF_ML + layer * ML_L + (x.b * 8 + x.g) * 256 + h * 64 + x.s * 8, v2u{nosent(fbits(M)), nosent(fbits(L))});
}

DI void wo_phase(Ctx& x, int layer, bool& alive) {
    refresh(x);
    const unsigned img = L_KW + x.w * 16384;
    v4u Bf[2][4];
    const int h = x.w >> 1;
    const int ooff = OFF_O + layer * O_L + ((x.b * 8 + x.g) * 8 + (x.cc >> 1)) * 1024 + h * 256 + (x.w & 1) * 128 + (x.cc & 1) * 64 + x.gp * 16;
    const int moff = OFF_ML + layer * ML_L + (x.b * 8 + x.g) * 256 + h * 64 + x.gp * 16;
    sleepn<DLY_O>();
    v4u A, ml;
    for (unsigned spin = 0;; ++spin) {
        MEMFENCE();
        A = ld128(x.ws, ooff); ml = ld128(x.ws, moff);
        if (wave_all(valid4(A) & valid4(ml))) break;
        if (!alive || spin > SPIN_MAX) { alive = false; break; }
    }
    MEMFENCE();
    read_wfrag(img, x.lane, Bf[0], 0);
    read_wfrag(img, x.lane, Bf[1], 1);
    f32x4 D0[4], D1[4];
#pragma unroll
    for (int n = 0; n < 4; ++n) { D0[n] = mfma32(A, Bf[0][n], f32x4{0.f, 0.f, 0.f, 0.f}); D1[n] = mfma32(A, Bf[1][n], f32x4{0.f, 0.f, 0.f, 0.f}); }
    const float ma = bitsf(ml.x), la = bitsf(ml.y), mb = bitsf(ml.z), lb = bitsf(ml.w);
    const float M = xrow_max(fmaxf(ma, mb));
    const float fa = ex2(ma - M), fb = ex2(mb - M);
    const float rl = rcpf(xrow_sum(fa * la + fb * lb));
    const float wa = fa * rl, wb = fb * rl;
    float y[4];
#pragma unroll
    for (int n = 0; n < 4; ++n) y[n] = wa * (D0[n][0] + D1[n][1]) + wb * (D0[n][2] + D1[n][3]);
    lds_st<float>(L_GW + x.w * 256 + x.lane * 4, xrow_scatter_sum(y[0], y[1], y[2], y[3]));
    BAR();
    if (x.tid < 16) {
        f32x4 t = lds_ld<f32x4>(L_GW + x.tid * 16);
#pragma unroll
        for (int w = 1; w < 8; ++w) t += lds_ld<f32x4>(L_GW + w * 256 + x.tid * 16);
        st128(x.ws, OFF_Y + layer * Y_L + ((x.b * 8 + x.g) * 8 + x.s) * 256 + x.tid * 16, v4u{nosent(fbits(t[0])), nosent(fbits(t[1])), nosent(fbits(t[2])), nosent(fbits(t[3]))});
    }
}

DI float mlp_phase(Ctx& x, int layer, float xprev, const v2u (&CF)[14], float b2, bool& alive) {
    refresh(x);
    const int yoff = OFF_Y + layer * Y_L + (x.b * 64 + x.w) * 256 + x.lane * 4;
    sleepn<DLY_Y>();
    unsigned yv[8];
    for (unsigned spin = 0;; ++spin) {
        MEMFENCE();
        bool ok = true;
#pragma unroll
        for (int g = 0; g < 8; ++g) { yv[g] = ld32(x.ws, yoff + g * 2048); ok &= yv[g] != SENT; }
        if (wave_all(ok)) break;
        if (!alive || spin > SPIN_MAX) { alive = false; break; }
    }
    MEMFENCE();
    const float ysum = ((bitsf(yv[0]) + bitsf(yv[1])) + (bitsf(yv[2]) + bitsf(yv[3]))) + ((bitsf(yv[4]) + bitsf(yv[5])) + (bitsf(yv[6]) + bitsf(yv[7])));
    const float xs = bfr(xprev + bfr(ysum));
    lds_st<float>(L_XROW + x.tid * 4, xs);
    const float s1 = xrow_sum(row_sum(xs)), s2 = xrow_sum(row_sum(xs * xs));
    if (x.lane == 0) lds_st<f32x2>(L_ST + x.w * 8, f32x2{s1, s2});
    BAR();
    const f32x4 t0 = lds_ld<f32x4>(L_ST), t1 = lds_ld<f32x4>(L_ST + 16), t2 = lds_ld<f32x4>(L_ST + 32), t3 = lds_ld<f32x4>(L_ST + 48);
    const int I = 8 * x.g + x.s;
    const int e = 2 * (x.cc >> 2) + (x.cc & 1);
    const float xe = lds_ld<float>(L_XROW + (8 * I + e) * 4);
    const float xo = lds_ld<float>(L_XROW + (8 * I + (x.lane & 7)) * 4);
    const float S1 = ((t0[0] + t0[2]) + (t1[0] + t1[2])) + ((t2[0] + t2[2]) + (t3[0] + t3[2]));
    const float S2 = ((t0[1] + t0[3]) + (t1[1] + t1[3])) + ((t2[1] + t2[3]) + (t3[1] + t3[3]));
    const float mu = S1 * (1.f / 512.f);
    const float var = fmaxf(S2 * (1.f / 512.f) - mu * mu, 0.f);
    const float r1 = rsqf(var + EPS);
    const float r2 = rsqf(var * r1 * r1 + EPS);
    const float h1 = (xe - mu) * (r1 * r2), h2 = h1 * h1, h3 = h2 * h1;
    const unsigned a1 = cvtpk(h1, h2), a3 = cvtpk(h3, h3);
    const unsigned H1h = a1 & 0xFFFFu, H2h = a1 >> 16, H3h = a3 & 0xFFFFu;
    const unsigned l12 = cvtpk(h1 - bf2f(H1h), h2 - bf2f(H2h)), l3 = cvtpk(h3 - bf2f(H3h), 0.f);
    const unsigned H1l = l12 & 0xFFFFu, H2l = l12 >> 16, H3l = l3 & 0xFFFFu;
    constexpr unsigned ONE = 0x3F80u;
    const bool grow = (x.cc >> 1) & 1;
    const unsigned ra = sel4u(x.gp, H3h | (H3h << 16), H2h | (H2l << 16), H1l | (ONE << 16), 0u), rb = sel4u(x.gp, H3l | (H2h << 16), H1h | (H1h << 16), ONE, 0u);
    const unsigned ga = sel4u(x.gp, 0u, 0u, 0u, H1h | (H1l << 16)), gb = sel4u(x.gp, 0u, 0u, H1h << 16, ONE | (ONE << 16));
    const unsigned r0 = grow ? ga : ra, r1_ = grow ? gb : rb;
    const v2u A = {r0, r1_};
    float acc0 = 0.f, acc1 = 0.f;
#pragma unroll
    for (int t = 0; t < 14; ++t) {
        const f32x4 D = mfma16(A, CF[t], f32x4{0.f, 0.f, 0.f, 0.f});
        acc0 = fmaf(D[2], rcpf(1.f + ex2(D[0])), acc0);
        acc1 = fmaf(D[3], rcpf(1.f + ex2(D[1])), acc1);
    }
    acc0 = row_sum(acc0); acc1 = row_sum(acc1);
    if (x.cc == 0) { lds_st<float>(L_MRED + ((2 * x.gp) * 8 + x.w) * 4, acc0); lds_st<float>(L_MRED + ((2 * x.gp + 1) * 8 + x.w) * 4, acc1); }
    BAR();
    float res = 0.f;
    if (x.w == 0) {
        const f32x4 u0 = lds_ld<f32x4>(L_MRED + (x.lane & 7) * 32), u1 = lds_ld<f32x4>(L_MRED + (x.lane & 7) * 32 + 16);
        const float y = ((u0[0] + u0[1]) + (u0[2] + u0[3])) + ((u1[0] + u1[1]) + (u1[2] + u1[3]));
        res = bfr(xo + bfr(bfr(y) + b2));
    }
    return res;
}
DI v4u pack8(float v) {
    const unsigned lo = fbits(v) >> 16;
    const unsigned pr = lo | (fbits(dpp<XOR1>(v)) & 0xFFFF0000u);
    return v4u{(unsigned)__builtin_amdgcn_readlane((int)pr, 0), (unsigned)__builtin_amdgcn_readlane((int)pr, 2), (unsigned)__builtin_amdgcn_readlane((int)pr, 4), (unsigned)__builtin_amdgcn_readlane((int)pr, 6)};
}

__global__ void __launch_bounds__(512, 2)
mega_kernel(const u16* __restrict__ xin, const u16* __restrict__ kc, const u16* __restrict__ vc, const u16* __restrict__ wq, const u16* __restrict__ wo,
            const u16* __restrict__ w1, const u16* __restrict__ b1, const u16* __restrict__ w2, const u16* __restrict__ b2p, u16* __restrict__ out, char* ws) {
    Ctx x;
    x.c = blockIdx.x; x.g = x.c & 7; x.b = x.c >> 6; x.s = (x.c >> 3) & 7;
    x.tid = threadIdx.x; x.lane = x.tid & 63; x.w = __builtin_amdgcn_readfirstlane(x.tid >> 6); x.gp = x.lane >> 4; x.cc = x.lane & 15;
    x.ws = mk_rsrc(ws, WS_BYTES);
    const int w = x.w;
#define lane x.lane
    bool alive = true;
    STP(0);
    const unsigned region = L_KW + w * 16384;
    const rsrc_t rq = mk_rsrc(wq, 512 * 4096 * 2), ro = mk_rsrc(wo, 4096 * 512 * 2);
    const size_t kvbase = ((size_t)(x.b * 8 + x.g) * SK + WIN0 + x.s * 512 + w * 64) * DH;
    const rsrc_t rk = mk_rsrc(kc + kvbase, 16384), rv = mk_rsrc(vc + kvbase, 16384);
    const int rot = __builtin_amdgcn_readfirstlane(((w >> 1) + (x.c >> 3) + x.g) & 3);
    int toff[4];
#pragma unroll
    for (int t = 0; t < 4; ++t) toff[t] = ((t + rot) & 3) * 4096;

    if (x.tid == 0) lds_st<unsigned>(L_FLAG, 0u);
    unsigned E = 0;
    v4u XA[4];
    if (w == 0) {
        const v4u sv = {SENT, SENT, SENT, SENT};
        const int slot = (x.b * 8 + x.g) * 8 + x.s;
        st128(x.ws, OFF_O + slot * 1024 + lane * 16, sv);
        st128(x.ws, OFF_O + O_L + slot * 1024 + lane * 16, sv);
        if (lane < 32) st128(x.ws, OFF_Y + (lane >> 4) * Y_L + slot * 256 + (lane & 15) * 16, sv);
        if (lane < 8) { st64(x.ws, OFF_ML + (lane >> 2) * ML_L + (x.b * 8 + x.g) * 256 + (lane & 3) * 64 + x.s * 8, v2u{SENT, SENT}); st128(x.ws, OFF_Q2 + slot * 128 + lane * 16, sv); }
        if (lane == 0) st128(x.ws, OFF_X2 + x.b * 1024 + (8 * x.g + x.s) * 16, sv);
        E = ld32(x.ws, OFF_E);
#pragma unroll
        for (int i = 0; i < 4; ++i) XA[i] = *(const GAS v4u*)(xin + (x.cc & 3) * 512 + 128 * (x.cc >> 2) + 32 * i + 8 * x.gp);
        const int v0 = wimg_voff(lane, 0, 8192), v1 = wimg_voff(lane, 1, 8192);
        const int sb = (128 * x.b) * 8192 + (512 * x.g + 64 * x.s) * 2;
#pragma unroll
        for (int m = 0; m < 16; ++m) DMA_W(region + m * 1024, (m & 1) ? v1 : v0, rq, sb + m * 8 * 8192);
    }
    const float x0 = bf2f(xin[x.b * 512 + x.tid]);
    const float b2 = bf2f(b2p[0]);
    unsigned cw1[4], cb1[4], cw2[4];
#pragma unroll
    for (int q = 0; q < 4; ++q) {
        const int f = 224 * w + 64 * q + (q == 3 ? (lane & 31) : lane);
        cw1[q] = w1[f]; cb1[q] = b1[f]; cw2[q] = w2[f];
    }
    BAR();
    STP(1);

    v4u V[2][8];
    auto issue_k = [&]() {
        int vk[4];
#pragma unroll
        for (int m = 0; m < 4; ++m) vk[m] = x.gp * 256 + ((x.cc ^ x.gp ^ (4 * m)) << 4);
#pragma unroll
        for (int t = 0; t < 4; ++t)
            asm volatile("s_mov_b32 m0, %0\n\ts_nop 0\n\t"
                         "buffer_load_dwordx4 %1, %5, %6 offen" KV_AUX " lds\n\t"
                         "buffer_load_dwordx4 %2, %5, %6 offen offset:1024" KV_AUX " lds\n\t"
                         "buffer_load_dwordx4 %3, %5, %6 offen offset:2048" KV_AUX " lds\n\t"
                         "buffer_load_dwordx4 %4, %5, %6 offen offset:3072" KV_AUX " lds"
                         ::"s"(region + t * 4096), "v"(vk[0]), "v"(vk[1]), "v"(vk[2]), "v"(vk[3]), "s"(rk), "s"(toff[t]) : "memory");
    };
    auto issue_v = [&]() {
        const int vv = x.gp * 1024 + x.cc * 16;
#define VLD(d, so, imm) "buffer_load_dwordx4 %" #d ", %16, %17, %" #so " offen offset:" #imm KV_AUX "\n\t"
        asm volatile(VLD(0, 18, 0) VLD(1, 18, 256) VLD(2, 18, 512) VLD(3, 18, 768) VLD(4, 19, 0) VLD(5, 19, 256) VLD(6, 19, 512) VLD(7, 19, 768)
                     VLD(8, 20, 0) VLD(9, 20, 256) VLD(10, 20, 512) VLD(11, 20, 768) VLD(12, 21, 0) VLD(13, 21, 256) VLD(14, 21, 512) VLD(15, 21, 768)
                     : "=&v"(V[0][0]), "=&v"(V[0][1]), "=&v"(V[0][2]), "=&v"(V[0][3]), "=&v"(V[0][4]), "=&v"(V[0][5]), "=&v"(V[0][6]), "=&v"(V[0][7]),
                       "=&v"(V[1][0]), "=&v"(V[1][1]), "=&v"(V[1][2]), "=&v"(V[1][3]), "=&v"(V[1][4]), "=&v"(V[1][5]), "=&v"(V[1][6]), "=&v"(V[1][7])
                     : "v"(vv), "s"(rv), "s"(toff[0]), "s"(toff[1]), "s"(toff[2]), "s"(toff[3]) : "memory");
#undef VLD
    };

    v2u CF[14];
    auto make_coefs = [&]() {
        constexpr float C0 = -2.3022081981f, C1 = C0 * 0.044715f;
        const unsigned st = L_CST + w * 1024;
#pragma unroll
        for (int q = 0; q < 7; ++q) {
            const int src = q >> 1;
            unsigned a = cw1[src], bb = cb1[src], d = cw2[src];
            if (src < 3) {
                if (q & 1) { auto s1 = __builtin_amdgcn_permlane32_swap(a, a, false, false); a = s1[1]; auto s2 = __builtin_amdgcn_permlane32_swap(bb, bb, false, false); bb = s2[1]; auto s3 = __builtin_amdgcn_permlane32_swap(d, d, false, false); d = s3[1]; }
                else { auto s1 = __builtin_amdgcn_permlane32_swap(a, a, false, false); a = s1[0]; auto s2 = __builtin_amdgcn_permlane32_swap(bb, bb, false, false); bb = s2[0]; auto s3 = __builtin_amdgcn_permlane32_swap(d, d, false, false); d = s3[0]; }
            }
            const float wv = bf2f(a), bv = bf2f(bb), w2v = bf2f(d);
            const float A3 = C1 * wv * wv * wv, A2 = 3.f * C1 * wv * wv * bv, A1 = C0 * wv + 3.f * C1 * wv * bv * bv, A0 = C0 * bv + C1 * bv * bv * bv, G1 = wv * w2v, G0 = bv * w2v;
            const unsigned h32 = cvtpk(A3, A2), h10 = cvtpk(A1, A0), hg = cvtpk(G1, G0);
            const unsigned l32 = cvtpk(A3 - bf2f(h32 & 0xFFFFu), A2 - bf2f(h32 >> 16)), l10 = cvtpk(A1 - bf2f(h10 & 0xFFFFu), A0 - bf2f(h10 >> 16)),
                           lg = cvtpk(G1 - bf2f(hg & 0xFFFFu), G0 - bf2f(hg >> 16));
            const unsigned A3h = h32 & 0xFFFFu, A2h = h32 >> 16, A1h = h10 & 0xFFFFu, A0h = h10 >> 16, G1h = hg & 0xFFFFu, G0h = hg >> 16;
            const unsigned A3l = l32 & 0xFFFFu, A2l = l32 >> 16, A1l = l10 & 0xFFFFu, A0l = l10 >> 16, G1l = lg & 0xFFFFu, G0l = lg >> 16;
            const v4u lo8 = {A3h | (A3l << 16), A3h | (A2h << 16), A2l | (A2h << 16), A1h | (A1l << 16)};
            const v4u hi8 = {A1h | (A0h << 16), A0l | (G1h << 16), G1l | (G1h << 16), G0h | (G0l << 16)};
            lds_st<v4u>(st + (lane & 31) * 32 + (lane >> 5) * 16, lane < 32 ? lo8 : hi8);
            asm volatile("s_waitcnt lgkmcnt(0)" ::: "memory");
            CF[2 * q] = lds_ld<v2u>(st + x.cc * 32 + x.gp * 8);
            CF[2 * q + 1] = lds_ld<v2u>(st + (16 + x.cc) * 32 + x.gp * 8);
            asm volatile("s_waitcnt lgkmcnt(0)" : "+v"(CF[2 * q]), "+v"(CF[2 * q + 1])::"memory");
        }
    };

    if (w == 0) {
        asm volatile("s_waitcnt vmcnt(0)" ::: "memory");
        STP(2);
        const unsigned T = __builtin_amdgcn_readfirstlane(E) + TAGSTEP;
        const v4u ones = {0x3F803F80u, 0x3F803F80u, 0x3F803F80u, 0x3F803F80u};
        f32x4 d1 = {0.f, 0.f, 0.f, 0.f}, d2 = d1;
#pragma unroll
        for (int i = 0; i < 4; ++i) { d1 = mfma32(XA[i], ones, d1); d2 = mfma32(XA[i], XA[i], d2); }
        const int be = x.cc & 3;
        float s1[4];
#pragma unroll
        for (int r = 0; r < 4; ++r) s1[r] = xrow_sum(d1[r]);
        const float sum = sel4(be, s1[0], s1[1], s1[2], s1[3]);
        float dg = sel4(be, d2[0], d2[1], d2[2], d2[3]);
        dg = (x.cc >> 2) == x.gp ? dg : 0.f;
        dg += dpp<ROR4>(dg); dg += dpp<ROR8>(dg);
        const float sq = xrow_sum(dg);
        const float mu = sum * (1.f / 512.f);
        const float rs = rsqf(fmaxf(sq * (1.f / 512.f) - mu * mu, 0.f) + EPS);
        f32x4 D[4];
#pragma unroll
        for (int n = 0; n < 4; ++n) D[n] = f32x4{0.f, 0.f, 0.f, 0.f};
#pragma unroll
        for (int i = 0; i < 4; ++i) {
            v4u H;
#pragma unroll
            for (int q = 0; q < 4; ++q) H[q] = cvtpk((bf2f(XA[i][q] & 0xFFFFu) - mu) * rs, (bitsf(XA[i][q] & 0xFFFF0000u) - mu) * rs);
            v4u Bf[4];
            read_wfrag(region + (i >> 1) * 8192, lane, Bf, i & 1);
#pragma unroll
            for (int n = 0; n < 4; ++n) D[n] = mfma32(H, Bf[n], D[n]);
        }
        asm volatile("s_waitcnt lgkmcnt(0)" ::: "memory");
        if (lane == 0) st128(x.ws, OFF_READY + x.c * 16, v4u{T, T, T, T});
        if (x.gp == x.b) {
#pragma unroll
            for (int be2 = 0; be2 < 4; ++be2)
#pragma unroll
                for (int np = 0; np < 2; ++np)
                    st128(x.ws, OFF_Q1P + ((x.g * 4 + be2) * 32 + (x.b * 8 + x.s)) * 512 + np * 256 + x.cc * 16, v4u{fbits(D[2 * np][be2]), T, fbits(D[2 * np + 1][be2]), T});
        }
        issue_k();
        STP(3);
        v4u G[16];
        const int gb = OFF_Q1P + (x.g * 4 + x.b) * 16384 + lane * 16;
        for (unsigned spin = 0;; ++spin) {
            MEMFENCE();
            bool ok = true;
#pragma unroll
            for (int m = 0; m < 16; ++m) { G[m] = ld128(x.ws, gb + m * 1024); ok &= (G[m].y == T) & (G[m].w == T); }
            const v4u rd = ld128(x.ws, OFF_READY + (x.b * 64 + lane) * 16);
            ok &= (rd.x == T) & (rd.y == T) & (rd.z == T) & (rd.w == T);
            if (wave_all(ok)) break;
            if (spin > SPIN_MAX) { alive = false; break; }
            __builtin_amdgcn_s_sleep(4);
        }
        MEMFENCE();
#pragma unroll
        for (int mm = 0; mm < 4; ++mm) {
            const float q0 = (bitsf(G[mm].x) + bitsf(G[4 + mm].x)) + (bitsf(G[8 + mm].x) + bitsf(G[12 + mm].x));
            const float q1 = (bitsf(G[mm].z) + bitsf(G[4 + mm].z)) + (bitsf(G[8 + mm].z) + bitsf(G[12 + mm].z));
            const int col = 64 * (2 * mm + (lane >> 5)) + 32 * ((lane >> 4) & 1) + x.cc;
            const unsigned pk = cvtpk(q0, q1);
            lds_st<u16>(L_QF + col * 2, (u16)(pk & 0xFFFFu));
            lds_st<u16>(L_QF + (col + 16) * 2, (u16)(pk >> 16));
        }
        asm volatile("s_waitcnt lgkmcnt(0)" ::: "memory");
        if (lane == 0) __hip_atomic_store((LAS unsigned*)(size_t)L_FLAG, 1u, __ATOMIC_RELAXED, __HIP_MEMORY_SCOPE_WORKGROUP);
        issue_v();
        if (x.c == 0) {
            for (unsigned spin = 0;; ++spin) {
                MEMFENCE();
                bool ok = true;
#pragma unroll
                for (int m = 0; m < 4; ++m) { const v4u rd = ld128(x.ws, OFF_READY + (m * 64 + lane) * 16); ok &= (rd.x == T) & (rd.y == T) & (rd.z == T) & (rd.w == T); }
                if (wave_all(ok)) { if (lane == 0) __builtin_amdgcn_raw_buffer_store_b32(T, x.ws, OFF_E, 0, 16); break; }
                if (spin > SPIN_MAX) break;
                __builtin_amdgcn_s_sleep(8);
            }
        }
        STP(4);
    } else {
        issue_k();
        issue_v();
        STP(2);
    }

    refresh(x);
    asm volatile("s_waitcnt vmcnt(16)" ::: "memory");
    v4u K[4][4];
#pragma unroll
    for (int i = 0; i < 4; ++i) {
        const unsigned a = region + x.cc * 256 + (((4 * i + x.gp) ^ x.cc) << 4);
#pragma unroll
        for (int t = 0; t < 4; ++t) K[t][i] = lds_ld<v4u>(a + t * 4096);
    }
    SCHED();
    STP(5);
    if (w != 0) {
        for (unsigned spin = 0; __hip_atomic_load((LAS unsigned*)(size_t)L_FLAG, __ATOMIC_RELAXED, __HIP_MEMORY_SCOPE_WORKGROUP) == 0u; ++spin) {
            if (spin > 64 * SPIN_MAX) break;
            __builtin_amdgcn_s_sleep(2);
        }
        MEMFENCE();
    }
    v4u qf[4];
#pragma unroll
    for (int i = 0; i < 4; ++i) qf[i] = lds_ld<v4u>(L_QF + ((x.cc >> 2) * 128 + 32 * i + 8 * x.gp) * 2);
    SCHED();
    STP(6);
    v4u P[2];
    float m1, l1;
    qk_softmax(K, qf, P, m1, l1);
    STP(7);
    v4u Vt[2][8];
    f32x4 acc[8];
#pragma unroll
    for (int jj = 0; jj < 8; ++jj) acc[jj] = f32x4{0.f, 0.f, 0.f, 0.f};
    SCHED();
#pragma unroll
    for (int u = 0; u < 2; ++u) {
        if (u == 0) asm volatile("s_waitcnt vmcnt(8)" : "+v"(V[0][0]), "+v"(V[0][1]), "+v"(V[0][2]), "+v"(V[0][3]), "+v"(V[0][4]), "+v"(V[0][5]), "+v"(V[0][6]), "+v"(V[0][7]), "+v"(P[0]), "+v"(P[1])::"memory");
        else asm volatile("s_waitcnt vmcnt(0)" : "+v"(V[1][0]), "+v"(V[1][1]), "+v"(V[1][2]), "+v"(V[1][3]), "+v"(V[1][4]), "+v"(V[1][5]), "+v"(V[1][6]), "+v"(V[1][7]), "+v"(acc[7])::"memory");
        SCHED();
#pragma unroll
        for (int jj = 0; jj < 8; ++jj) {
#pragma unroll
            for (int q = 0; q < 4; ++q) Vt[u][jj][q] = __builtin_amdgcn_perm(V[u][2 * q + 1][jj >> 1], V[u][2 * q][jj >> 1], (jj & 1) ? 0x07060302u : 0x05040100u);
            acc[jj] = mfma32(P[u], Vt[u][jj], acc[jj]);
        }
        SCHED();
    }
    STP(8);
    refresh(x);
    {
        const int v0 = wimg_voff(lane, 0, 1024), v1 = wimg_voff(lane, 1, 1024);
        const int sb = (512 * x.g + 64 * w) * 1024 + 64 * x.s * 2;
#pragma unroll
        for (int m = 0; m < 8; ++m) DMA_W(region + m * 1024, (m & 1) ? v1 : v0, ro, sb + m * 8 * 1024);
    }
    merge_publish(x, 0, acc, m1, l1);
    STP(9);
    wo_phase(x, 0, alive);
    STP(10);
    refresh(x);
    {
        const int v0 = wimg_voff(lane, 0, 8192), v1 = wimg_voff(lane, 1, 8192);
        const int sb = (64 * w) * 8192 + (512 * x.g + 64 * x.s) * 2;
#pragma unroll
        for (int m = 0; m < 8; ++m) DMA_W(region + 8192 + m * 1024, (m & 1) ? v1 : v0, rq, sb + m * 8 * 8192);
    }
    refresh(x);
    make_coefs();
    {
        const float r = mlp_phase(x, 0, x0, CF, b2, alive);
        if (w == 0) {
            const v4u pk = pack8(r);
            if (lane == 0) st128(x.ws, OFF_X2 + x.b * 1024 + (8 * x.g + x.s) * 16, v4u{nosent(pk.x), nosent(pk.y), nosent(pk.z), nosent(pk.w)});
        }
    }
    STP(11);
    refresh(x);
    float x2v;
    {
        const int xoff = OFF_X2 + x.b * 1024 + (8 * w + 4 * (x.cc & 1) + x.gp) * 16;
        sleepn<DLY_X>();
        v4u A;
        for (unsigned spin = 0;; ++spin) {
            MEMFENCE();
            A = ld128(x.ws, xoff);
            if (wave_all(valid4(A))) break;
            if (!alive || spin > SPIN_MAX) { alive = false; break; }
        }
        MEMFENCE();
        STP(12);
        if (x.cc < 2) lds_st<v4u>(L_X2 + (64 * w + 32 * x.cc + 8 * x.gp) * 2, A);
        if (x.cc & 2) A = v4u{0x3F803F80u, 0x3F803F80u, 0x3F803F80u, 0x3F803F80u};
        v4u Bf[2][4];
        read_wfrag(region + 8192, lane, Bf[0], 0);
        read_wfrag(region + 8192, lane, Bf[1], 1);
        f32x4 D0[4], D1[4];
#pragma unroll
        for (int n = 0; n < 4; ++n) { D0[n] = mfma32(A, Bf[0][n], f32x4{0.f, 0.f, 0.f, 0.f}); D1[n] = mfma32(A, Bf[1][n], f32x4{0.f, 0.f, 0.f, 0.f}); }
        const f32x4 st = mfma32(A, A, f32x4{0.f, 0.f, 0.f, 0.f});
        float yq[4], cs[4];
#pragma unroll
        for (int n = 0; n < 4; ++n) { yq[n] = D0[n][0] + D1[n][1]; cs[n] = D0[n][2] + D1[n][3]; }
        const float y = sel4(x.gp, yq[0], yq[1], yq[2], yq[3]);
        const float z = sel4(x.gp, cs[0], cs[1], cs[2], cs[3]);
        lds_st<float>(L_GQ + w * 512 + lane * 4, y);
        lds_st<float>(L_GQ + w * 512 + 256 + lane * 4, z);
        if (lane < 2) lds_st<f32x2>(L_ST + 64 + (w * 2 + lane) * 8, f32x2{st[2], lane == 0 ? st[0] : st[1]});
        BAR();
        x2v = bf2f(lds_ld<u16>(L_X2 + x.tid * 2));
        if (w == 0) {
            float a = 0.f, cz = 0.f;
#pragma unroll
            for (int ww = 0; ww < 8; ++ww) { a += lds_ld<float>(L_GQ + ww * 512 + lane * 4); cz += lds_ld<float>(L_GQ + ww * 512 + 256 + lane * 4); }
            const f32x2 t = lds_ld<f32x2>(L_ST + 64 + x.cc * 8);
            const float mu = row_sum(t[0]) * (1.f / 512.f);
            const float rs = rsqf(fmaxf(row_sum(t[1]) * (1.f / 512.f) - mu * mu, 0.f) + EPS);
            const float q = (a - mu * cz) * rs;
            const unsigned pr = cvtpk(q, dpp<XOR1>(q));
            const unsigned p2 = fbits(dpp<0x4E>(bitsf(pr)));
            if ((lane & 3) == 0) st64(x.ws, OFF_Q2 + ((x.b * 8 + x.g) * 8 + x.s) * 128 + lane * 2, v2u{nosent(pr), nosent(p2)});
        }
    }
    STP(13);
    refresh(x);
    {
        const int qoff = OFF_Q2 + (x.b * 8 + x.g) * 1024 + ((x.cc >> 2) * 128 + 8 * x.gp) * 2;
        sleepn<DLY_Q>();
        for (unsigned spin = 0;; ++spin) {
            MEMFENCE();
            bool ok = true;
#pragma unroll
            for (int i = 0; i < 4; ++i) { qf[i] = ld128(x.ws, qoff + i * 64); ok &= valid4(qf[i]); }
            if (wave_all(ok)) break;
            if (!alive || spin > SPIN_MAX) { alive = false; break; }
        }
        MEMFENCE();
    }
    STP(14);
    refresh(x);
    float m2, l2;
    qk_softmax(K, qf, P, m2, l2);
#pragma unroll
    for (int jj = 0; jj < 8; ++jj) {
        acc[jj] = mfma32(P[0], Vt[0][jj], f32x4{0.f, 0.f, 0.f, 0.f});
        acc[jj] = mfma32(P[1], Vt[1][jj], acc[jj]);
    }
    STP(15);
    merge_publish(x, 1, acc, m2, l2);
    STP(16);
    wo_phase(x, 1, alive);
    STP(17);
    {
        const float r = mlp_phase(x, 1, x2v, CF, b2, alive);
        if (w == 0) {
            const v4u pk = pack8(r);
            if (lane == 0) *(GAS v4u*)(out + x.b * 512 + (8 * x.g + x.s) * 8) = pk;
        }
    }
    STP(18);
}

#undef lane
}

extern "C" void kernel_launch(void* const* d_in, const int* in_sizes, int n_in, void* d_out, int out_size, void* d_ws, size_t ws_size, hipStream_t stream) {
    (void)in_sizes; (void)n_in; (void)out_size; (void)ws_size;
    hipFuncSetAttribute((const void*)mega_kernel, hipFuncAttributeMaxDynamicSharedMemorySize, LDS_BYTES);
    mega_kernel<<<256, 512, LDS_BYTES, stream>>>((const u16*)d_in[0], (const u16*)d_in[1], (const u16*)d_in[2], (const u16*)d_in[3], (const u16*)d_in[4],
                                                 (const u16*)d_in[6], (const u16*)d_in[7], (const u16*)d_in[8], (const u16*)d_in[9], (u16*)d_out, (char*)d_ws);
}
```

```cpp
#include <hip/hip_runtime.h>
#include <stdint.h>

#define GAS __attribute__((address_space(1)))
#define LAS __attribute__((address_space(3)))
#define DI __device__ __forceinline__
typedef unsigned v4u __attribute__((ext_vector_type(4)));
typedef unsigned v2u __attribute__((ext_vector_type(2)));
typedef unsigned v8u __attribute__((ext_vector_type(8)));
typedef float f32x4 __attribute__((ext_vector_type(4)));
typedef float f32x2 __attribute__((ext_vector_type(2)));
typedef __bf16 bf16x2 __attribute__((ext_vector_type(2)));
typedef unsigned short u16;

namespace {

#define NT " nt"
constexpr int NK0 = 3;
constexpr int NV0 = 4;
constexpr int QPOLL = 0;
constexpr int WO_N1 = 4, WO_N1S = 8;
constexpr int WO_SLOW = 3;
constexpr int WQ_K0 = 1, WQ_SPAN = 22;
constexpr int DLY_O = 0, DLY_O0 = 2, DLY_Y = 6, DLY_Y1 = 3, DLY_X = 1, DLY_X1 = 4, DLY_Q = 3, DLY_Q1 = 10;

constexpr int SK = 16384, WIN0 = 12288, DH = 128;
constexpr unsigned SENT = 0xFFFFFFFFu;
constexpr unsigned TAGSTEP = 0x9E3779B9u;
constexpr unsigned SPIN_MAX = 1u << 15;
constexpr float EPS = 1e-5f;
constexpr float SCL = 0.08838834764831845f * 1.4426950408889634f;

constexpr int OFF_E = 0, OFF_READY = 0x8000, OFF_Q1P = 0x10000, OFF_O = 0x100000, OFF_ML = 0x180000, OFF_Y = 0x190000, OFF_X2 = 0x1B0000,
              OFF_Q2 = 0x1C0000, WS_BYTES = 0x200000;
constexpr int O_L = 0x40000, ML_L = 0x2000, Y_L = 0x10000;
constexpr int L_KW = 0, L_S16 = 131072, L_RED = L_S16, L_GQ = L_S16 + 8192, L_GW = L_S16 + 10240, L_RAW = 147456, L_ML = 158208, L_ST = 158336,
              L_STQ = 158400, L_MRED = 158464, L_DUMMY = 158592, L_DUMMY16 = 158608, L_QF = 158720, L_XROW = 159744, L_X2 = 160768,
                L_QP = L_S16, L_QC = L_S16 + 1024, L_QS = L_S16 + 1280, L_TAG = L_S16 + 1408,
              LDS_BYTES = 163840;

DI unsigned fbits(float x) { return __builtin_bit_cast(unsigned, x); }
DI float bitsf(unsigned x) { return __builtin_bit_cast(float, x); }
DI float bflo(unsigned w) { return bitsf(w << 16); }
DI float bfhi(unsigned w) { return bitsf(w & 0xFFFF0000u); }
DI unsigned cvtpk(float lo, float hi) { f32x2 v = {lo, hi}; return __builtin_bit_cast(unsigned, __builtin_convertvector(v, bf16x2)); }
DI float bfr(float x) { return bflo(cvtpk(x, x)); }
DI unsigned nosent(unsigned x) { return x < 0xFFFFFFFEu ? x : 0xFFFFFFFEu; }
DI unsigned umax3(unsigned a, unsigned b, unsigned c) { const unsigned m = a > b ? a : b; return m > c ? m : c; }
DI unsigned umax4(v4u v, unsigned m) { return umax3(umax3(v.x, v.y, m), v.z, v.w); }
template <int CTRL> DI float dpp(float x) { return bitsf(__builtin_amdgcn_mov_dpp((int)fbits(x), CTRL, 0xf, 0xf, true)); }
constexpr int ROR1 = 0x121, ROR2 = 0x122, ROR4 = 0x124, ROR8 = 0x128, XOR1 = 0xB1, XOR2 = 0x4E;
DI float vmax(float a, float b) { float r; asm("v_max_f32 %0, %1, %2" : "=v"(r) : "v"(a), "v"(b)); return r; }
DI float vadd(float a, float b) { float r; asm("v_add_f32 %0, %1, %2" : "=v"(r) : "v"(a), "v"(b)); return r; }
DI float vmax3(float a, float b, float c) { float r; asm("v_max3_f32 %0, %1, %2, %3" : "=v"(r) : "v"(a), "v"(b), "v"(c)); return r; }
DI float xrow_max(float x) {
    auto s = __builtin_amdgcn_permlane16_swap(fbits(x), fbits(x), false, false);
    x = vmax(bitsf(s[0]), bitsf(s[1]));
    auto t = __builtin_amdgcn_permlane32_swap(fbits(x), fbits(x), false, false);
    return vmax(bitsf(t[0]), bitsf(t[1]));
}
DI float xrow_sum(float x) {
    auto s = __builtin_amdgcn_permlane16_swap(fbits(x), fbits(x), false, false);
    x = bitsf(s[0]) + bitsf(s[1]);
    auto t = __builtin_amdgcn_permlane32_swap(fbits(x), fbits(x), false, false);
    return bitsf(t[0]) + bitsf(t[1]);
}
DI float row_sum(float x) { x += dpp<ROR8>(x); x += dpp<ROR4>(x); x += dpp<ROR2>(x); x += dpp<ROR1>(x); return x; }
DI float xrow_scatter_sum(float y0, float y1, float y2, float y3) {
    auto a = __builtin_amdgcn_permlane32_swap(fbits(y0), fbits(y2), false, false);
    auto b = __builtin_amdgcn_permlane32_swap(fbits(y1), fbits(y3), false, false);
    const float z0 = bitsf(a[0]) + bitsf(a[1]), z1 = bitsf(b[0]) + bitsf(b[1]);
    auto c = __builtin_amdgcn_permlane16_swap(fbits(z0), fbits(z1), false, false);
    return bitsf(c[0]) + bitsf(c[1]);
}
DI float pin(float v) { asm volatile("" : "+v"(v)); return v; }
DI float sel4(int k, float a, float b, float c, float d) { a = pin(a); b = pin(b); c = pin(c); d = pin(d); const float lo = (k & 1) ? b : a, hi = (k & 1) ? d : c; return (k & 2) ? hi : lo; }
DI float ex2(float x) { return __builtin_amdgcn_exp2f(x); }
DI float rcpf(float x) { return __builtin_amdgcn_rcpf(x); }
DI float rsqf(float x) { return __builtin_amdgcn_rsqf(x); }
#define BAR() asm volatile("s_waitcnt lgkmcnt(0)\n\ts_barrier" ::: "memory")
#define MEMFENCE() asm volatile("" ::: "memory")
#define SCHED() __builtin_amdgcn_sched_barrier(0)
typedef __amdgpu_buffer_rsrc_t rsrc_t;
DI rsrc_t mk_rsrc(const void* p, unsigned bytes) { return __builtin_amdgcn_make_buffer_rsrc((void*)p, 0, bytes, 0x00020000); }
DI v4u ld128(rsrc_t r, int off) { return __builtin_amdgcn_raw_buffer_load_b128(r, off, 0, 16); }
DI v2u ld64(rsrc_t r, int off) { return __builtin_amdgcn_raw_buffer_load_b64(r, off, 0, 16); }
DI unsigned ld32(rsrc_t r, int off) { return __builtin_amdgcn_raw_buffer_load_b32(r, off, 0, 16); }
DI void st128(rsrc_t r, int off, v4u v) { __builtin_amdgcn_raw_buffer_store_b128(v, r, off, 0, 16); }
DI void st64(rsrc_t r, int off, v2u v) { __builtin_amdgcn_raw_buffer_store_b64(v, r, off, 0, 16); }
DI void st32(rsrc_t r, int off, unsigned v) { __builtin_amdgcn_raw_buffer_store_b32(v, r, off, 0, 16); }
DI bool any_sent(unsigned mx) { return __builtin_amdgcn_ballot_w64(mx == SENT) != 0ull; }
template <typename T> DI T lds_ld(unsigned a) { return *(const LAS T*)(size_t)a; }
template <typename T> DI void lds_st(unsigned a, T v) { *(LAS T*)(size_t)a = v; }
DI int fresh_lane() { int l = __builtin_amdgcn_mbcnt_hi(~0u, __builtin_amdgcn_mbcnt_lo(~0u, 0u)); asm volatile("" : "+v"(l)); return l; }
#define MF32 "v_mfma_f32_16x16x32_bf16 "
#define MF16 "v_mfma_f32_16x16x16_bf16 "

DI int wimg_voff(int lane, int odd, int rowstride) {
    const int r = lane >> 3, phi = ((r >> 1) & 1) | (odd << 1);
    return r * rowstride + (((lane & 7) ^ (2 * phi)) << 4);
}
#define DMA16(m0v, voff, rs, soff, aux) asm volatile("s_mov_b32 m0, %0\n\ts_nop 0\n\tbuffer_load_dwordx4 %1, %2, %3 offen" aux " lds" ::"s"(m0v), "v"(voff), "s"(rs), "s"(soff) : "memory")
template <int ROWSTRIDE, int M0, int NP> DI void dma_wimg(unsigned dst, int lane, rsrc_t rs, int sbase) {
    static_assert(M0 % 2 == 0 && NP % 2 == 0 && M0 + NP <= 16, "pairs of pieces");
    if constexpr (NP > 0) {
        const int v0 = wimg_voff(lane, 0, ROWSTRIDE), v1 = wimg_voff(lane, 1, ROWSTRIDE);
        int so = __builtin_amdgcn_readfirstlane(sbase + M0 * 8 * ROWSTRIDE);
        int m0v = __builtin_amdgcn_readfirstlane(dst + M0 * 1024);
#pragma unroll
        for (int p = 0; p < NP / 2; ++p)
            asm volatile("s_mov_b32 m0, %1\n\ts_nop 0\n\tbuffer_load_dwordx4 %2, %4, %0 offen" NT " lds\n\ts_add_u32 m0, m0, 0x400\n\ts_add_u32 %0, %0, %5\n\t"
                         "buffer_load_dwordx4 %3, %4, %0 offen" NT " lds\n\ts_add_u32 %0, %0, %5\n\ts_add_u32 %1, %1, 0x800"
                         : "+s"(so), "+s"(m0v) : "v"(v0), "v"(v1), "s"(rs), "i"(8 * ROWSTRIDE) : "memory", "scc");
    }
}
template <int ROWSTRIDE> DI void dma_wpiece(unsigned dst, int v0, int v1, rsrc_t rs, int sbase, int m) {
    DMA16(dst + m * 1024, (m & 1) ? v1 : v0, rs, sbase + m * 8 * ROWSTRIDE, NT);
}
typedef short s16x4 __attribute__((ext_vector_type(4)));
DI v2u lds_tr(unsigned a) { return __builtin_bit_cast(v2u, __builtin_amdgcn_ds_read_tr16_b64_v4i16((LAS s16x4*)(size_t)a)); }
struct WAddr { unsigned a[4]; };
DI WAddr wfrag_addr(unsigned img, int lane) {
    const int gp = lane >> 4, r = (lane >> 2) & 3, cc = lane & 3;
    const int phi = ((r >> 1) & 1) | ((gp & 1) << 1);
    WAddr A;
#pragma unroll
    for (int n = 0; n < 4; ++n) A.a[n] = img + (8 * gp + r) * 128 + cc * 8 + ((n ^ phi) << 5);
    asm volatile("" : "+v"(A.a[0]), "+v"(A.a[1]), "+v"(A.a[2]), "+v"(A.a[3]));
    return A;
}
DI void read_wfrags(const WAddr& A, v8u (&F)[2][4]) {
#pragma unroll
    for (int bk = 0; bk < 2; ++bk)
#pragma unroll
        for (int n = 0; n < 4; ++n) {
            const unsigned a = A.a[n] + bk * 8192;
            const v2u p0 = lds_tr(a), p1 = lds_tr(a + 512), p2 = lds_tr(a + 4096), p3 = lds_tr(a + 4608);
            F[bk][n] = v8u{p0.x, p0.y, p1.x, p1.y, p2.x, p2.y, p3.x, p3.y};
        }
}

struct Ctx {
    int c, g, b, s, w;
    rsrc_t ws;
};

DI void qk_tile(f32x4& S, const v4u (&K)[4], const v4u (&q)[4]) {
    asm(MF32 "%0, %1, %5, 0\n\t" MF32 "%0, %2, %6, %0\n\t" MF32 "%0, %3, %7, %0\n\t" MF32 "%0, %4, %8, %0\n\ts_nop 1"
        : "=&v"(S) : "a"(K[0]), "a"(K[1]), "a"(K[2]), "a"(K[3]), "v"(q[0]), "v"(q[1]), "v"(q[2]), "v"(q[3]));
}
DI void qk_tile_max(f32x4& S, float& m, const f32x4& Sp, const v4u (&K)[4], const v4u (&q)[4]) {
    asm(MF32 "%0, %6, %10, 0\n\t" MF32 "%0, %7, %11, %0\n\tv_max3_f32 %1, %2, %3, %1\n\t" MF32 "%0, %8, %12, %0\n\tv_max3_f32 %1, %4, %5, %1\n\t" MF32 "%0, %9, %13, %0"
        : "=&v"(S), "+v"(m) : "v"(Sp[0]), "v"(Sp[1]), "v"(Sp[2]), "v"(Sp[3]), "a"(K[0]), "a"(K[1]), "a"(K[2]), "a"(K[3]), "v"(q[0]), "v"(q[1]), "v"(q[2]), "v"(q[3]));
}
template <bool FIRST> DI void pv_step(f32x4& acc, const v4u& P, const v4u& V) {
    if constexpr (FIRST) asm(MF32 "%0, %1, %2, 0" : "=&a"(acc) : "v"(P), "v"(V));
    else asm(MF32 "%0, %1, %2, %0" : "+a"(acc) : "v"(P), "v"(V));
}

DI float qk_all(f32x4 (&S)[8], const v4u (&K)[8][4], const v4u (&q)[4]) {
    float m = -3.0e38f;
    SCHED();
    asm volatile("s_nop 1");
    qk_tile(S[0], K[0], q); SCHED();
    qk_tile(S[1], K[1], q); SCHED();
#pragma unroll
    for (int t = 2; t < 8; ++t) { qk_tile_max(S[t], m, S[t - 2], K[t], q); SCHED(); }
    asm volatile("s_nop 7" : "+v"(S[6]), "+v"(S[7]));
    m = vmax3(vmax3(S[6][0], S[6][1], m), S[6][2], S[6][3]);
    m = vmax3(vmax3(S[7][0], S[7][1], m), S[7][2], S[7][3]);
    return xrow_max(m) * SCL;
}
DI v4u make_p(const f32x4& a, const f32x4& b, float ms) {
    float e[8];
#pragma unroll
    for (int r = 0; r < 4; ++r) { e[r] = ex2(fmaf(a[r], SCL, -ms)); e[4 + r] = ex2(fmaf(b[r], SCL, -ms)); }
    return v4u{cvtpk(e[0], e[1]), cvtpk(e[2], e[3]), cvtpk(e[4], e[5]), cvtpk(e[6], e[7])};
}

struct MAddr { unsigned red, mlw, ra; int oo, mo; };
DI MAddr merge_addr(const Ctx& x, int layer) {
    const int lane = fresh_lane(), gp = lane >> 4, cc = lane & 15;
    const int slot = (x.b * 8 + x.g) * 8 + x.s;
    MAddr A;
    A.red = L_RED + x.w * 2048 + gp * 512 + cc * 32;
    A.mlw = cc == 4 * gp ? L_ML + (gp * 4 + x.w) * 8 : L_DUMMY;
    A.ra = L_RED + x.w * 512 + lane * 8;
    A.oo = OFF_O + layer * O_L + slot * 1024 + x.w * 256 + ((lane & ~7) + 4 * (lane & 1) + ((lane >> 1) & 3)) * 4;
    A.mo = lane == 0 ? OFF_ML + layer * ML_L + (x.b * 8 + x.g) * 256 + x.w * 64 + x.s * 8 : 0x7FFFFFF0;
    asm volatile("" : "+v"(A.red), "+v"(A.mlw), "+v"(A.ra), "+v"(A.oo), "+v"(A.mo));
    return A;
}
DI void merge_publish(Ctx& x, f32x4 (&acc)[9], float m, const MAddr& A) {
    asm volatile("s_nop 7" : "+a"(acc[0]), "+a"(acc[1]), "+a"(acc[2]), "+a"(acc[3]), "+a"(acc[4]), "+a"(acc[5]), "+a"(acc[6]), "+a"(acc[7]), "+a"(acc[8]));
    lds_st<f32x4>(A.red, f32x4{acc[0][0], acc[1][0], acc[2][0], acc[3][0]});
    lds_st<f32x4>(A.red + 16, f32x4{acc[4][0], acc[5][0], acc[6][0], acc[7][0]});
    lds_st<f32x2>(A.mlw, f32x2{m, acc[8][0]});
    BAR();
    const f32x4 ml0 = lds_ld<f32x4>(L_ML + x.w * 32), ml1 = lds_ld<f32x4>(L_ML + x.w * 32 + 16);
    const f32x2 o0 = lds_ld<f32x2>(A.ra), o1 = lds_ld<f32x2>(A.ra + 2048), o2 = lds_ld<f32x2>(A.ra + 4096), o3 = lds_ld<f32x2>(A.ra + 6144);
    const float M = vmax3(vmax(ml0[0], ml0[2]), ml1[0], ml1[2]);
    const float f0 = ex2(ml0[0] - M), f1 = ex2(ml0[2] - M), f2 = ex2(ml1[0] - M), f3 = ex2(ml1[2] - M);
    const f32x2 o = (o0 * f0 + o1 * f1) + (o2 * f2 + o3 * f3);
    st32(x.ws, A.oo, nosent(cvtpk(o[0], o[1])));
    const float L = fmaf(f3, ml1[3], fmaf(f2, ml1[1], fmaf(f1, ml0[3], pin(f0 * ml0[1]))));
    st64(x.ws, A.mo, v2u{nosent(fbits(M)), nosent(fbits(L))});
}

#define SMF "v_smfmac_f32_16x16x64_bf16 "
DI void smf_step(f32x4& D, const v4u& A, const v8u& B, unsigned idx) { asm(SMF "%0, %1, %2, %3" : "+v"(D) : "v"(A), "v"(B), "v"(idx)); }
#define PIN_D4(D) "+v"(D[0]), "+v"(D[1]), "+v"(D[2]), "+v"(D[3])

template <bool LATE, typename Post> DI void wo_phase(Ctx& x, int layer, unsigned Tag, Post post) {
    const int lane = fresh_lane(), gp = lane >> 4, cc = lane & 15;
    v8u F[2][4];
    const WAddr WA = wfrag_addr(L_KW + x.w * 32768, lane);
    if (!LATE) read_wfrags(WA, F);
    const int ooff = OFF_O + layer * O_L + ((x.b * 8 + x.g) * 8 + (cc >> 1)) * 1024 + x.w * 256 + gp * 32 + (cc & 1) * 16;
    const int moff = OFF_ML + layer * ML_L + (x.b * 8 + x.g) * 256 + x.w * 64 + gp * 16;
    unsigned idx = (cc & 1) ? 0xEEEEu : 0x4444u;
    unsigned agw = L_GW + x.w * 256 + lane * 4, agr = L_GW + lane * 4;
    int yo = OFF_Y + layer * Y_L + ((x.b * 8 + x.g) * 8 + x.s) * 256 + lane * 4;
    f32x4 D[4];
#pragma unroll
    for (int n = 0; n < 4; ++n) D[n] = f32x4{0.f, 0.f, 0.f, 0.f};
    asm volatile("" : PIN_D4(D), "+v"(idx), "+v"(agw), "+v"(agr), "+v"(yo));
    if (LATE) __builtin_amdgcn_s_sleep(DLY_O0); else if (DLY_O > 0) __builtin_amdgcn_s_sleep(DLY_O);
    v4u A0, A1, ml, rdy = {0, 0, 0, 0};
    if (layer == 0 && x.w == 2) rdy = ld128(x.ws, OFF_READY + (x.b * 64 + lane) * 16);
    unsigned spin = 0;
    for (; spin < SPIN_MAX; ++spin) {
        MEMFENCE();
        A0 = ld128(x.ws, ooff); A1 = ld128(x.ws, ooff + 128); ml = ld128(x.ws, moff);
        if (!any_sent(umax4(A0, umax4(A1, umax4(ml, 0u))))) break;
    }
    MEMFENCE();
    post();
    if (LATE) read_wfrags(WA, F);
    const float ma = bitsf(ml.x), la = bitsf(ml.y), mb = bitsf(ml.z), lb = bitsf(ml.w);
    asm volatile("s_nop 1" : "+v"(A0), "+v"(A1));
    SCHED();
    smf_step(D[0], A0, F[0][0], idx); SCHED();
    float M = vmax(ma, mb); SCHED();
    smf_step(D[1], A0, F[0][1], idx); SCHED();
    M = xrow_max(M); SCHED();
    smf_step(D[2], A0, F[0][2], idx); SCHED();
    smf_step(D[3], A0, F[0][3], idx); SCHED();
    const float fa = ex2(ma - M), fb = ex2(mb - M); SCHED();
    smf_step(D[0], A1, F[1][0], idx); SCHED();
    float L = fa * la + fb * lb; SCHED();
    smf_step(D[1], A1, F[1][1], idx); SCHED();
    smf_step(D[2], A1, F[1][2], idx); SCHED();
    L = xrow_sum(L); SCHED();
    smf_step(D[3], A1, F[1][3], idx); SCHED();
    const float rl = rcpf(L);
    float wa = fa * rl, wb = fb * rl;
    asm volatile("s_nop 5" : PIN_D4(D), "+v"(wa), "+v"(wb));
    float y[4];
#pragma unroll
    for (int n = 0; n < 4; ++n) y[n] = pin(fmaf(wb, vadd(D[n][2], D[n][3]), pin(wa * vadd(D[n][0], D[n][1]))));
    lds_st<float>(agw, xrow_scatter_sum(y[0], y[1], y[2], y[3]));
    if (layer == 0 && x.w == 2) {
        unsigned late = (rdy.x ^ Tag) | (rdy.y ^ Tag) | (rdy.z ^ Tag) | (rdy.w ^ Tag);
        for (unsigned sp = 0; __builtin_amdgcn_ballot_w64(late != 0u) != 0ull && sp < SPIN_MAX; ++sp) {
            MEMFENCE();
            const v4u rd = ld128(x.ws, OFF_READY + (x.b * 64 + lane) * 16);
            late = (rd.x ^ Tag) | (rd.y ^ Tag) | (rd.z ^ Tag) | (rd.w ^ Tag);
        }
        MEMFENCE();
    }
    BAR();
    if (x.w == 0) {
        const float t = (lds_ld<float>(agr) + lds_ld<float>(agr + 256)) + (lds_ld<float>(agr + 512) + lds_ld<float>(agr + 768));
        st32(x.ws, yo, nosent(fbits(t)));
    }
}

template <typename Side> DI float mlp_phase(Ctx& x, int layer, unsigned xprev, const v2u (&CF)[28], unsigned b2raw, Side side) {
    const int lane = fresh_lane(), gp = lane >> 4, cc = lane & 15;
    const int c4 = lane & 31, half = lane >> 5;
    const int yoff = OFF_Y + layer * Y_L + (x.b * 64 + 2 * x.w + (c4 >> 4)) * 256 + (c4 & 15) * 16 + half * 8192;
    const int I = 8 * x.g + x.s;
    const int cb = 16 * (I >> 1) + 2 * (I & 1);
    unsigned aw = L_XROW + (128 * x.w + 4 * c4 + 2 * half) * 2, ae = L_XROW + (cb + 4 * (cc >> 2) + (cc & 1)) * 2, ao = L_XROW + (cb + 4 * ((lane >> 1) & 3) + (lane & 1)) * 2;
    unsigned am = L_MRED + ((2 * gp) * 4 + x.w) * 4, ar = L_MRED + (lane & 7) * 16;
    const bool grow = (cc >> 1) & 1;
    float live = (grow ? gp == 3 : gp < 3) ? 1.f : 0.f, one = (grow ? gp >= 2 : gp < 2) ? 1.f : 0.f;
    float kA = gp < 2 ? 1.f : 0.f, kB = 1.f - kA, kC = gp == 0 ? 1.f : 0.f, kD = 1.f - kC;
    float xp0 = bflo(xprev), xp1 = bfhi(xprev);
    asm volatile("" : "+v"(aw), "+v"(ae), "+v"(ao), "+v"(am), "+v"(ar), "+v"(live), "+v"(one), "+v"(kA), "+v"(kB), "+v"(kC), "+v"(kD), "+v"(xp0), "+v"(xp1));
    if (x.w == 0) __builtin_amdgcn_s_sleep(DLY_Y); else __builtin_amdgcn_s_sleep(DLY_Y + DLY_Y1);
    v4u yv[4];
    unsigned spin = 0;
    for (; spin < SPIN_MAX; ++spin) {
        MEMFENCE();
        unsigned mx = 0;
#pragma unroll
        for (int g = 0; g < 4; ++g) { yv[g] = ld128(x.ws, yoff + g * 2048); mx = umax4(yv[g], mx); }
        if (!any_sent(mx)) break;
    }
    MEMFENCE();
    float ya, yb2;
    {
        float y4[4];
#pragma unroll
        for (int q = 0; q < 4; ++q) y4[q] = pin((bitsf(yv[0][q]) + bitsf(yv[1][q])) + (bitsf(yv[2][q]) + bitsf(yv[3][q])));
        auto a = __builtin_amdgcn_permlane32_swap(fbits(y4[0]), fbits(y4[2]), false, false);
        auto b = __builtin_amdgcn_permlane32_swap(fbits(y4[1]), fbits(y4[3]), false, false);
        ya = bitsf(a[0]) + bitsf(a[1]); yb2 = bitsf(b[0]) + bitsf(b[1]);
    }
    const unsigned yb = cvtpk(ya, yb2);
    const unsigned xs = cvtpk(xp0 + bflo(yb), xp1 + bfhi(yb));
    lds_st<unsigned>(aw, xs);
    const float s1 = xrow_sum(row_sum(__builtin_amdgcn_fdot2_f32_bf16(__builtin_bit_cast(bf16x2, xs), __builtin_bit_cast(bf16x2, 0x3F803F80u), 0.f, false)));
    const float s2 = xrow_sum(row_sum(__builtin_amdgcn_fdot2_f32_bf16(__builtin_bit_cast(bf16x2, xs), __builtin_bit_cast(bf16x2, xs), 0.f, false)));
    if (lane == 0) lds_st<f32x2>(L_ST + x.w * 8, f32x2{s1, s2});
    BAR();
    f32x4 t0 = lds_ld<f32x4>(L_ST), t1 = lds_ld<f32x4>(L_ST + 16);
    unsigned xer = lds_ld<u16>(ae), xor_ = lds_ld<u16>(ao);
    asm volatile("" : "+v"(t0), "+v"(t1), "+v"(xer), "+v"(xor_));
    const float xe = bflo(xer);
    const f32x2 S = ((f32x2{t0[0], t0[1]} + f32x2{t0[2], t0[3]}) + (f32x2{t1[0], t1[1]} + f32x2{t1[2], t1[3]})) * (1.f / 512.f);
    const float mu = S[0];
    const float var = fmaxf(S[1] - mu * mu, 0.f);
    const float r1 = rsqf(var + EPS);
    const float r2 = rsqf(var * r1 * r1 + EPS);
    const float h = (xe - mu) * (r1 * r2);
    const float P = (h * live) * fmaf(kA, h, kB) * fmaf(kC, h, kD);
    const unsigned a0 = cvtpk(P, P);
    v2u A = {a0, cvtpk(P - bflo(a0), one)};
    float acc0 = 0.f, acc1 = 0.f;
    {
        f32x4 D[6];
        float E0[2], E1[2], R0[2], R1[2];
        asm volatile("s_nop 1" : "+v"(A));
        asm(MF16 "%0, %3, %4, 0\n\t" MF16 "%1, %3, %5, 0\n\t" MF16 "%2, %3, %6, 0\n\ts_nop 6" : "=&v"(D[0]), "=&v"(D[1]), "=&v"(D[2]) : "v"(A), "a"(CF[0]), "a"(CF[1]), "a"(CF[2]));
        SCHED();
        E0[0] = ex2(D[0][0]); E1[0] = ex2(D[0][1]);
        SCHED();
#pragma unroll
        for (int k = 0; k < 29; ++k) {
            if (k + 3 < 28) asm(MF16 "%0, %1, %2, 0" : "=&v"(D[(k + 3) % 6]) : "v"(A), "a"(CF[k + 3 < 28 ? k + 3 : 0]));
            SCHED();
            float d0 = 0.f, d1 = 0.f;
            if (k < 28) { asm("v_add_f32 %0, 1.0, %1" : "=v"(d0) : "v"(E0[k & 1])); asm("v_add_f32 %0, 1.0, %1" : "=v"(d1) : "v"(E1[k & 1])); }
            SCHED();
            if (k + 1 < 28) E0[(k + 1) & 1] = ex2(D[(k + 1) % 6][0]);
            SCHED();
            if (k < 28) R0[k & 1] = rcpf(d0);
            SCHED();
            if (k + 1 < 28) E1[(k + 1) & 1] = ex2(D[(k + 1) % 6][1]);
            SCHED();
            if (k < 28) R1[k & 1] = rcpf(d1);
            SCHED();
            if (k >= 1) { asm("v_fmac_f32 %0, %1, %2" : "+v"(acc0) : "v"(D[(k - 1) % 6][2]), "v"(R0[(k - 1) & 1])); asm("v_fmac_f32 %0, %1, %2" : "+v"(acc1) : "v"(D[(k - 1) % 6][3]), "v"(R1[(k - 1) & 1])); }
            if (k >= WQ_K0 && (k - WQ_K0) * 16 / WQ_SPAN != (k - WQ_K0 + 1) * 16 / WQ_SPAN && (k - WQ_K0) * 16 / WQ_SPAN < 16) side((k - WQ_K0) * 16 / WQ_SPAN);
            SCHED();
        }
    }
    acc0 = row_sum(acc0); acc1 = row_sum(acc1);
    if (cc == 0) { lds_st<float>(am, acc0); lds_st<float>(am + 16, acc1); }
    BAR();
    float res = 0.f;
    if (x.w == 0) {
        const f32x4 u = lds_ld<f32x4>(ar);
        res = bfr(bflo(xor_) + bfr(bfr((u[0] + u[1]) + (u[2] + u[3])) + bflo(b2raw)));
    }
    return res;
}
DI unsigned pack2(float v) { return (fbits(v) >> 16) | (fbits(dpp<XOR1>(v)) & 0xFFFF0000u); }

__global__ void __launch_bounds__(256, 1)
mega_kernel(const u16* __restrict__ xin, const u16* __restrict__ kc, const u16* __restrict__ vc, const u16* __restrict__ wq, const u16* __restrict__ wo,
            const u16* __restrict__ w1, const u16* __restrict__ b1, const u16* __restrict__ w2, const u16* __restrict__ b2p, u16* __restrict__ out, char* ws) {
    Ctx x;
    x.c = blockIdx.x; x.g = x.c & 7; x.b = x.c >> 6; x.s = (x.c >> 3) & 7;
    x.w = __builtin_amdgcn_readfirstlane(threadIdx.x >> 6);
    x.ws = mk_rsrc(ws, WS_BYTES);
    const int w = x.w;
    const unsigned region = L_KW + w * 32768;
    const rsrc_t rq = mk_rsrc(wq, 512 * 4096 * 2), ro = mk_rsrc(wo, 4096 * 512 * 2);
    const size_t kvbase = ((size_t)(x.b * 8 + x.g) * SK + WIN0 + x.s * 512 + w * 128) * DH;
    const rsrc_t rk = mk_rsrc(kc + kvbase, 32768), rv = mk_rsrc(vc + kvbase, 32768);
    const int rot = (2 * w + (x.c >> 3) + x.g) & 7;

    v4u V[4][8];
    int vk[4], vv;
    {
        const int lane = fresh_lane(), gp = lane >> 4, cc = lane & 15;
#pragma unroll
        for (int m = 0; m < 4; ++m) vk[m] = gp * 256 + ((cc ^ gp ^ (4 * m)) << 4);
        vv = gp * 1024 + cc * 16;
    }
    auto issue_k = [&](int t) {
        asm volatile("s_mov_b32 m0, %0\n\ts_nop 0\n\t"
                     "buffer_load_dwordx4 %1, %5, %6 offen" NT " lds\n\t"
                     "buffer_load_dwordx4 %2, %5, %6 offen offset:1024" NT " lds\n\t"
                     "buffer_load_dwordx4 %3, %5, %6 offen offset:2048" NT " lds\n\t"
                     "buffer_load_dwordx4 %4, %5, %6 offen offset:3072" NT " lds"
                     ::"s"(__builtin_amdgcn_readfirstlane(region + t * 4096)), "v"(vk[0]), "v"(vk[1]), "v"(vk[2]), "v"(vk[3]), "s"(rk), "s"(((t + rot) & 7) * 4096) : "memory");
    };
    auto issue_v = [&](int t) {
        v4u (&D)[8] = V[t >> 1];
        const int o = 4 * (t & 1);
        asm volatile("buffer_load_dwordx4 %0, %4, %5, %6 offen" NT "\n\tbuffer_load_dwordx4 %1, %4, %5, %6 offen offset:256" NT "\n\t"
                     "buffer_load_dwordx4 %2, %4, %5, %6 offen offset:512" NT "\n\tbuffer_load_dwordx4 %3, %4, %5, %6 offen offset:768" NT
                     : "=&v"(D[o]), "=&v"(D[o + 1]), "=&v"(D[o + 2]), "=&v"(D[o + 3]) : "v"(vv), "s"(rv), "s"(((t + rot) & 7) * 4096) : "memory");
    };
    auto issue_raw = [&](int lane) {
        if (lane < 56) {
            const unsigned dst = L_RAW + w * 2688;
            const int so = w * 896;
            DMA16(dst, lane * 16, mk_rsrc(w1, 3584), so, "");
            DMA16(dst + 896, lane * 16, mk_rsrc(b1, 3584), so, "");
            DMA16(dst + 1792, lane * 16, mk_rsrc(w2, 3584), so, "");
        }
    };
    v2u CF[28];
    v2u RW[3][7];
    auto read_raw = [&]() {
        const int lane = fresh_lane();
        const unsigned src = L_RAW + w * 2688 + (lane & 15) * 56;
#pragma unroll
        for (int a = 0; a < 3; ++a)
#pragma unroll
            for (int i = 0; i < 7; ++i) RW[a][i] = lds_ld<v2u>(src + a * 896 + i * 8);
    };
    auto make_coef = [&](int t, int gp) {
        constexpr float C0 = -2.3022081981f, C1 = C0 * 0.044715f;
        const unsigned pw = RW[0][t >> 2][(t >> 1) & 1], pb = RW[1][t >> 2][(t >> 1) & 1], p2 = RW[2][t >> 2][(t >> 1) & 1];
        const float wv = (t & 1) ? bfhi(pw) : bflo(pw), bv = (t & 1) ? bfhi(pb) : bflo(pb), w2v = (t & 1) ? bfhi(p2) : bflo(p2);
        const float bb = bv * bv;
        const float t0 = gp == 0 ? C1 * wv : 3.f * C1 * bv;
        const float t1 = gp == 2 ? C0 + 3.f * C1 * bb : w2v;
        const float X = wv * (gp < 2 ? wv * t0 : t1);
        const float Y = bv * (gp < 2 ? C0 + C1 * bb : w2v);
        const unsigned xh = cvtpk(X, Y);
        const unsigned xl = cvtpk(X - bflo(xh), Y - bfhi(xh));
        CF[t] = v2u{__builtin_amdgcn_perm(xl, xh, 0x05040100u), __builtin_amdgcn_perm((gp & 1) ? xl : xh, xh, 0x07060100u)};
        asm volatile("" : "+a"(CF[t]));
    };
    v4u K[8][4];
    auto read_k = [&]() {
        const int lane = fresh_lane(), gp = lane >> 4, cc = lane & 15;
#pragma unroll
        for (int i = 0; i < 4; ++i) {
            const unsigned a = region + cc * 256 + (((4 * i + gp) ^ cc) << 4);
#pragma unroll
            for (int t = 0; t < 8; ++t) asm volatile("ds_read_b128 %0, %1 offset:%2" : "=a"(K[t][i]) : "v"(a), "i"(t * 4096) : "memory");
        }
#define PINK(t) "+a"(K[t][0]), "+a"(K[t][1]), "+a"(K[t][2]), "+a"(K[t][3])
        asm volatile("s_waitcnt lgkmcnt(0)" : PINK(0), PINK(1), PINK(2), PINK(3)::"memory");
        asm volatile("" : PINK(4), PINK(5), PINK(6), PINK(7)::"memory");
#undef PINK
    };

    unsigned b2 = 0;
    unsigned Tag;
    {
        const int lane = fresh_lane(), gp = lane >> 4, cc = lane & 15;
        {
            const int v0 = wimg_voff(lane, 0, 8192), v1 = wimg_voff(lane, 1, 8192);
            const int sb = (128 * x.b + 32 * w) * 8192 + (512 * x.g + 64 * x.s) * 2;
#pragma unroll
            for (int m = 0; m < 4; ++m) dma_wpiece<8192>(L_S16 + w * 4096, v0, v1, rq, sb, m);
        }
        unsigned E = 0;
        v4u XA;
        asm volatile("global_load_dwordx4 %0, %1, off" : "=&v"(XA) : "v"((const GAS char*)(xin + (cc & 3) * 512 + 128 * (cc >> 2) + 32 * w + 8 * gp)) : "memory");
        if (w == 0) asm volatile("global_load_dword %0, %2, off\n\tglobal_load_ushort %1, %3, off" : "=&v"(E), "=&v"(b2) : "v"((const GAS char*)ws + OFF_E), "v"((const GAS char*)b2p) : "memory");
        unsigned touch = 0;
        if (w == 3 && lane < 8) {
            const GAS char* p = lane == 0 ? (const GAS char*)(vc + kvbase) : lane == 1 ? (const GAS char*)(out + x.b * 512) : lane == 2 ? (const GAS char*)wq : lane == 3 ? (const GAS char*)wq + (2 << 20)
                              : lane == 4 ? (const GAS char*)(wo + (size_t)512 * x.g * 512) : lane == 5 ? (const GAS char*)w1 : lane == 6 ? (const GAS char*)b1 : (const GAS char*)w2;
            asm volatile("global_load_dword %0, %1, off nt" : "=&v"(touch) : "v"(p) : "memory");
        }
        {
            const v4u sv = {SENT, SENT, SENT, SENT};
            const int slot = (x.b * 8 + x.g) * 8 + x.s;
            auto sentinels = [&]() {
                if (w < 2) st128(x.ws, OFF_O + w * O_L + slot * 1024 + lane * 16, sv);
                else if (w == 2) {
                    const int off = lane < 32 ? OFF_Y + (lane >> 4) * Y_L + slot * 256 + (lane & 15) * 16 : lane < 40 ? OFF_Q2 + slot * 128 + (lane - 32) * 16 : OFF_X2 + x.b * 1024 + (8 * x.g + x.s) * 16;
                    if (lane <= 40) st128(x.ws, off, sv);
                } else if (lane < 8) st64(x.ws, OFF_ML + (lane >> 2) * ML_L + (x.b * 8 + x.g) * 256 + (lane & 3) * 64 + x.s * 8, v2u{SENT, SENT});
            };
            sentinels();
#pragma unroll
            for (int t = 0; t < NK0; ++t) issue_k(t);
        }
        asm volatile("s_waitcnt vmcnt(%3)" : "+v"(E), "+v"(XA), "+v"(b2) : "i"(4 * NK0) : "memory");
        asm volatile("" :: "v"(touch));
        {
            v4u F[4];
            {
                const int r = (lane >> 2) & 3, c4 = lane & 3, phi = ((r >> 1) & 1) | ((gp & 1) << 1);
#pragma unroll
                for (int n = 0; n < 4; ++n) {
                    const unsigned a = L_S16 + w * 4096 + (8 * gp + r) * 128 + c4 * 8 + ((n ^ phi) << 5);
                    const v2u lo = lds_tr(a), hi = lds_tr(a + 512);
                    F[n] = v4u{lo.x, lo.y, hi.x, hi.y};
                }
            }
            v4u ones = {0x3F803F80u, 0x3F803F80u, 0x3F803F80u, 0x3F803F80u};
            v4u H = (cc >> 2) == ((x.b + 1) & 3) ? ones : XA;
            f32x4 d1, d2, D[4];
            asm("s_nop 1\n\t" MF32 "%0, %6, %8, 0\n\t" MF32 "%1, %6, %6, 0\n\t" MF32 "%2, %7, %9, 0\n\t" MF32 "%3, %7, %10, 0\n\t" MF32 "%4, %7, %11, 0\n\t" MF32 "%5, %7, %12, 0\n\ts_nop 7"
                : "=&v"(d1), "=&v"(d2), "=&v"(D[0]), "=&v"(D[1]), "=&v"(D[2]), "=&v"(D[3]) : "v"(XA), "v"(H), "v"(ones), "v"(F[0]), "v"(F[1]), "v"(F[2]), "v"(F[3]));
            issue_k(NK0);
            if ((cc & 3) == x.b) lds_st<v4u>(L_X2 + (128 * (cc >> 2) + 32 * w + 8 * gp) * 2, XA);
            if (gp == x.b) {
#pragma unroll
                for (int n = 0; n < 4; ++n) lds_st<f32x4>(L_QP + w * 4096 + n * 256 + cc * 16, D[n]);
            }
            if (gp == ((x.b + 1) & 3)) lds_st<f32x4>(L_QC + w * 4096 + cc * 16, f32x4{D[0][0], D[1][0], D[2][0], D[3][0]});
            if (cc == 0) lds_st<f32x4>(L_QS + w * 4096 + gp * 16, d1);
            if ((cc >> 2) == gp) lds_st<float>(L_QS + w * 4096 + 64 + cc * 4, sel4(cc & 3, d2[0], d2[1], d2[2], d2[3]));
            if (w == 0 && lane == 0) lds_st<unsigned>(L_TAG, E + TAGSTEP);
        }
        issue_k(NK0 + 1);
        BAR();
        Tag = __builtin_amdgcn_readfirstlane(lds_ld<unsigned>(L_TAG));
        const unsigned T = Tag;
        {
            const unsigned sa = L_QS + (cc >> 2) * 4096 + ((cc & 3) * 4 + w) * 4;
            const float s1 = row_sum(lds_ld<float>(sa)), s2 = row_sum(lds_ld<float>(sa + 64));
            const unsigned a = L_QP + (2 * (gp & 1)) * 256 + cc * 16 + w * 4;
            const float v0 = (lds_ld<float>(a) + lds_ld<float>(a + 4096)) + (lds_ld<float>(a + 8192) + lds_ld<float>(a + 12288));
            const float v1 = (lds_ld<float>(a + 256) + lds_ld<float>(a + 4352)) + (lds_ld<float>(a + 8448) + lds_ld<float>(a + 12544));
            const unsigned ca = L_QC + cc * 16 + (gp & 1) * 8;
            const f32x2 cs = (lds_ld<f32x2>(ca) + lds_ld<f32x2>(ca + 4096)) + (lds_ld<f32x2>(ca + 8192) + lds_ld<f32x2>(ca + 12288));
            const float mu = s1 * (1.f / 512.f);
            const float rs = rsqf(fmaxf(s2 * (1.f / 512.f) - mu * mu, 0.f) + EPS);
            const unsigned p = cvtpk((v0 - mu * cs[0]) * rs, (v1 - mu * cs[1]) * rs);
            auto pp = __builtin_amdgcn_permlane16_swap(p, p, false, false);
            const bool rdy = w == 0 && lane == 16;
            const int po = rdy ? OFF_READY + x.c * 16 : OFF_Q1P + (((x.g * 4 + w) * 4 + (x.s >> 1)) * 4 + x.b) * 512 + (x.s & 1) * 256 + cc * 16;
            if (lane < 16 || rdy) st128(x.ws, po, rdy ? v4u{T, T, T, T} : v4u{pp[0], T, pp[1], T});
        }
        issue_raw(lane);
#pragma unroll
        for (int t = NK0 + 2; t < 8; ++t) {
            issue_k(t);
            SCHED();
        }
        asm volatile("s_waitcnt vmcnt(%0)" ::"i"(4 * (6 - NK0)) : "memory");
        read_raw();
#pragma unroll
        for (int q = 0; q < 4; ++q) make_coef(q, gp);
        v4u G[2];
#pragma unroll
        for (int j = 0; j < NV0; ++j) {
            issue_v(j);
            if (j == QPOLL) {
#pragma unroll
                for (int m = 0; m < 2; ++m) asm volatile("buffer_load_dwordx4 %0, %1, %2, 0 offen sc1" : "=&v"(G[m]) : "v"(OFF_Q1P + ((x.g * 4 + x.b) * 4 + w) * 2048 + m * 1024 + lane * 16), "s"(x.ws) : "memory");
            }
            {
                constexpr int per = (24 + NV0 - 1) / NV0;
#pragma unroll
                for (int q = 0; q < per; ++q) if (4 + j * per + q < 28) make_coef(4 + j * per + q, gp);
            }
            SCHED();
        }
        asm volatile("s_waitcnt vmcnt(%0)" ::"i"(4 * NV0 + 2) : "memory");
        read_k();
        {
            asm volatile("s_waitcnt vmcnt(%2)" : "+v"(G[0]), "+v"(G[1]) : "i"(4 * (NV0 - 1 - QPOLL)) : "memory");
            unsigned bad = (G[0].y ^ T) | (G[0].w ^ T) | (G[1].y ^ T) | (G[1].w ^ T);
            for (unsigned spin = 0; __builtin_amdgcn_ballot_w64(bad != 0u) != 0ull && spin < SPIN_MAX; ++spin) {
                MEMFENCE();
                bad = 0;
#pragma unroll
                for (int m = 0; m < 2; ++m) { G[m] = ld128(x.ws, OFF_Q1P + ((x.g * 4 + x.b) * 4 + w) * 2048 + m * 1024 + lane * 16); bad |= (G[m].y ^ T) | (G[m].w ^ T); }
            }
            MEMFENCE();
            const float t0 = bflo(G[0].x) + bflo(G[1].x), t1 = bfhi(G[0].x) + bfhi(G[1].x), t2 = bflo(G[0].z) + bflo(G[1].z), t3 = bfhi(G[0].z) + bfhi(G[1].z);
            auto a02 = __builtin_amdgcn_permlane32_swap(fbits(t0), fbits(t2), false, false);
            auto a13 = __builtin_amdgcn_permlane32_swap(fbits(t1), fbits(t3), false, false);
            const unsigned pk = cvtpk(bitsf(a02[0]) + bitsf(a02[1]), bitsf(a13[0]) + bitsf(a13[1]));
            const int col = 64 * (2 * w + (gp & 1)) + 32 * (lane >> 5) + cc;
            lds_st<u16>(L_QF + col * 2, (u16)(pk & 0xFFFFu));
            lds_st<u16>(L_QF + (col + 16) * 2, (u16)(pk >> 16));
        }
    }
    BAR();
    v4u qf[4];
    {
        const int lane = fresh_lane();
#pragma unroll
        for (int i = 0; i < 4; ++i) qf[i] = lds_ld<v4u>(L_QF + (((lane & 15) >> 2) * 128 + 32 * i + 8 * (lane >> 4)) * 2);
    }
    v4u ones4 = {0x3F803F80u, 0x3F803F80u, 0x3F803F80u, 0x3F803F80u};
    asm volatile("" : "+v"(ones4));
    v4u Vt[4][8];
    f32x4 acc[9];
    const MAddr MA0 = merge_addr(x, 0);
    float m1;
    {
        f32x4 S[8];
#pragma unroll
        for (int j = NV0; j < 6; ++j) issue_v(j);
        m1 = qk_all(S, K, qf);
        SCHED();
        issue_v(6);
        v4u P[4];
        P[0] = make_p(S[0], S[1], m1); P[1] = make_p(S[2], S[3], m1);
        SCHED();
        issue_v(7);
        P[2] = make_p(S[4], S[5], m1); P[3] = make_p(S[6], S[7], m1);
        asm volatile("" : "+v"(P[0]), "+v"(P[1]), "+v"(P[2]), "+v"(P[3]));
        SCHED();
#pragma unroll
        for (int u = 0; u < 4; ++u) {
#define PINV(u) "+v"(V[u][0]), "+v"(V[u][1]), "+v"(V[u][2]), "+v"(V[u][3]), "+v"(V[u][4]), "+v"(V[u][5]), "+v"(V[u][6]), "+v"(V[u][7])
            if (u == 0) asm volatile("s_waitcnt vmcnt(24)" : PINV(0)::"memory");
            if (u == 1) asm volatile("s_waitcnt vmcnt(16)" : PINV(1)::"memory");
            if (u == 3) {
                if (x.s == WO_SLOW) dma_wimg<1024, 0, WO_N1S>(region, fresh_lane(), ro, (512 * x.g + 128 * w) * 1024 + 64 * x.s * 2);
                else dma_wimg<1024, 0, WO_N1>(region, fresh_lane(), ro, (512 * x.g + 128 * w) * 1024 + 64 * x.s * 2);
            }
            if (u == 2) asm volatile("s_waitcnt vmcnt(8)" : PINV(2)::"memory");
            if (u == 3) {
                asm volatile("s_cmp_eq_u32 %8, %c11\n\ts_cbranch_scc1 .Lsl_%=\n\ts_waitcnt vmcnt(%c9)\n\ts_branch .Lse_%=\n.Lsl_%=:\n\ts_waitcnt vmcnt(%c10)\n.Lse_%=:"
                             : PINV(3) : "s"(x.s), "i"(WO_N1), "i"(WO_N1S), "i"(WO_SLOW) : "memory", "scc");
            }
#undef PINV
            SCHED();
            auto perms = [&](int jj) {
#pragma unroll
                for (int q = 0; q < 4; ++q) Vt[u][jj][q] = __builtin_amdgcn_perm(V[u][2 * q + 1][jj >> 1], V[u][2 * q][jj >> 1], (jj & 1) ? 0x07060302u : 0x05040100u);
            };
            perms(0);
            SCHED();
            if (u == 0) pv_step<true>(acc[8], P[u], ones4); else pv_step<false>(acc[8], P[u], ones4);
            SCHED();
#pragma unroll
            for (int jj = 0; jj < 8; ++jj) {
                if (jj < 7) perms(jj + 1); else asm volatile("s_nop 1");
                SCHED();
                if (u == 0) pv_step<true>(acc[jj], P[u], Vt[u][jj]); else pv_step<false>(acc[jj], P[u], Vt[u][jj]);
                SCHED();
            }
        }
    }
    auto wo_rest = [&]() {
        if (x.s == WO_SLOW) dma_wimg<1024, WO_N1S, 16 - WO_N1S>(region, fresh_lane(), ro, (512 * x.g + 128 * w) * 1024 + 64 * x.s * 2);
        else dma_wimg<1024, WO_N1, 16 - WO_N1>(region, fresh_lane(), ro, (512 * x.g + 128 * w) * 1024 + 64 * x.s * 2);
    };
    merge_publish(x, acc, m1, MA0);
    wo_rest();
    unsigned pf = 0;
    auto wq_touch = [&]() {
        if (w < 2) {
            const int l4 = fresh_lane();
            asm volatile("buffer_load_dword %0, %1, %2, 0 offen nt" : "=&v"(pf) : "v"((16 * (x.c >> 3) + 8 * w + (l4 >> 3)) * 8192 + x.g * 1024 + (l4 & 7) * 128), "s"(rq) : "memory");
        }
    };
    wo_phase<true>(x, 0, Tag, wq_touch);
    {
        const int l2 = fresh_lane();
        const int v0 = wimg_voff(l2, 0, 8192), v1 = wimg_voff(l2, 1, 8192);
        int sb = __builtin_amdgcn_readfirstlane((128 * w) * 8192 + (512 * x.g + 64 * x.s) * 2);
        int dst = __builtin_amdgcn_readfirstlane(region + 16384);
        asm volatile("" : "+s"(sb), "+s"(dst));
        const unsigned x0 = lds_ld<unsigned>(L_X2 + (128 * w + 4 * (l2 & 31) + 2 * (l2 >> 5)) * 2);
        int x2off = OFF_X2 + x.b * 1024 + (8 * x.g + x.s) * 16 + l2 * 2;
        asm volatile("" : "+v"(x2off));
        const float r = mlp_phase(x, 0, x0, CF, b2, [&](int m) { dma_wpiece<8192>(dst, v0, v1, rq, sb, m); });
        if (w == 0) {
            const unsigned pk = pack2(r);
            if ((l2 & ~6) == 0) st32(x.ws, x2off, nosent(pk));
        }
    }
    asm volatile("" :: "v"(pf));
    unsigned x2v;
    {
        const int lane = fresh_lane(), gp = lane >> 4, cc = lane & 15;
        v8u F[2][4];
        if (w == 0) asm volatile("s_waitcnt vmcnt(1)" ::: "memory"); else asm volatile("s_waitcnt vmcnt(0)" ::: "memory");
        read_wfrags(wfrag_addr(region + 16384, lane), F);
        const int xoff = OFF_X2 + x.b * 1024 + 256 * w + 32 * gp + 16 * (cc & 1);
        unsigned idx = (cc & 1) ? 0xEEEEu : 0x4444u;
        unsigned ax = cc < 2 ? L_X2 + 256 * w + 32 * gp + 16 * cc : L_DUMMY16, ax1 = cc < 2 ? ax + 128 : L_DUMMY16, as = lane < 2 ? L_STQ + (w * 2 + lane) * 8 : L_DUMMY;
        unsigned ag = L_GQ + w * 512 + lane * 4, agr = L_GQ + lane * 4, asr = L_STQ + (lane & 7) * 8;
        unsigned a2 = L_X2 + (64 * w + 8 * ((lane & 31) >> 2) + 4 * (lane >> 5) + (lane & 3)) * 4;
        int qo = (lane & 3) == 0 ? OFF_Q2 + ((x.b * 8 + x.g) * 8 + x.s) * 128 + lane * 2 : 0x7FFFFFF0;
        f32x4 D[4];
#pragma unroll
        for (int n = 0; n < 4; ++n) D[n] = f32x4{0.f, 0.f, 0.f, 0.f};
        asm volatile("" : PIN_D4(D), "+v"(idx), "+v"(ax), "+v"(ax1), "+v"(as), "+v"(ag), "+v"(agr), "+v"(asr), "+v"(a2), "+v"(qo));
        if (w == 0) __builtin_amdgcn_s_sleep(DLY_X); else __builtin_amdgcn_s_sleep(DLY_X + DLY_X1);
        v4u A0, A1;
        unsigned spin = 0;
        for (; spin < SPIN_MAX; ++spin) {
            MEMFENCE();
            A0 = ld128(x.ws, xoff); A1 = ld128(x.ws, xoff + 128);
            if (!any_sent(umax4(A0, umax4(A1, 0u)))) break;
        }
        MEMFENCE();
        const v4u B0 = (cc & 2) ? ones4 : A0, B1 = (cc & 2) ? ones4 : A1;
        v4u C0 = B0, C1 = B1;
        f32x4 st;
        asm volatile("s_nop 1" : "+v"(C0), "+v"(C1));
        SCHED();
        asm(MF32 "%0, %1, %1, 0\n\t" MF32 "%0, %2, %2, %0" : "=&v"(st) : "v"(C0), "v"(C1));
        SCHED();
        smf_step(D[0], C0, F[0][0], idx); SCHED();
        lds_st<v4u>(ax, A0);
        SCHED();
        smf_step(D[1], C0, F[0][1], idx); SCHED();
        lds_st<v4u>(ax1, A1);
        SCHED();
        smf_step(D[2], C0, F[0][2], idx); smf_step(D[3], C0, F[0][3], idx); SCHED();
        asm volatile("" : "+v"(st));
        smf_step(D[0], C1, F[1][0], idx); smf_step(D[1], C1, F[1][1], idx); SCHED();
        lds_st<f32x2>(as, f32x2{st[2], (lane & 1) ? st[1] : st[0]});
        SCHED();
        smf_step(D[2], C1, F[1][2], idx); smf_step(D[3], C1, F[1][3], idx); SCHED();
        asm volatile("s_nop 7" : PIN_D4(D));
        const f32x4 Dg = (gp & 2) ? ((gp & 1) ? D[3] : D[2]) : ((gp & 1) ? D[1] : D[0]);
        lds_st<float>(ag, vadd(Dg[0], Dg[1]));
        lds_st<float>(ag + 256, vadd(Dg[2], Dg[3]));
        BAR();
        x2v = lds_ld<unsigned>(a2);
        if (w == 0) {
            const float a = (lds_ld<float>(agr) + lds_ld<float>(agr + 512)) + (lds_ld<float>(agr + 1024) + lds_ld<float>(agr + 1536));
            const float cz = (lds_ld<float>(agr + 256) + lds_ld<float>(agr + 768)) + (lds_ld<float>(agr + 1280) + lds_ld<float>(agr + 1792));
            const f32x2 t = lds_ld<f32x2>(asr);
            float s1 = t[0], s2 = t[1];
            s1 += dpp<ROR4>(s1); s2 += dpp<ROR4>(s2); s1 += dpp<ROR2>(s1); s2 += dpp<ROR2>(s2); s1 += dpp<ROR1>(s1); s2 += dpp<ROR1>(s2);
            const float mu = s1 * (1.f / 512.f);
            const float rs = rsqf(fmaxf(s2 * (1.f / 512.f) - mu * mu, 0.f) + EPS);
            const float q = (a - mu * cz) * rs;
            const unsigned pr = cvtpk(q, dpp<XOR1>(q));
            const unsigned p2 = fbits(dpp<XOR2>(bitsf(pr)));
            st64(x.ws, qo, v2u{nosent(pr), nosent(p2)});
        }
    }
    const MAddr MA1 = merge_addr(x, 1);
    {
        const int lane = fresh_lane();
        const int qoff = OFF_Q2 + (x.b * 8 + x.g) * 1024 + (((lane & 15) >> 2) * 128 + 32 * (lane & 3) + 8 * (lane >> 4)) * 2;
        if (w == 0) __builtin_amdgcn_s_sleep(DLY_Q); else __builtin_amdgcn_s_sleep(DLY_Q + DLY_Q1);
        unsigned spin = 0;
        v4u Q;
        for (; spin < SPIN_MAX; ++spin) {
            MEMFENCE();
            Q = ld128(x.ws, qoff);
            if (!any_sent(umax4(Q, 0u))) break;
        }
        MEMFENCE();
#pragma unroll
        for (int r = 0; r < 4; ++r) {
            qf[0][r] = (unsigned)__builtin_amdgcn_mov_dpp((int)Q[r], 0x00, 0xf, 0xf, true);
            qf[1][r] = (unsigned)__builtin_amdgcn_mov_dpp((int)Q[r], 0x55, 0xf, 0xf, true);
            qf[2][r] = (unsigned)__builtin_amdgcn_mov_dpp((int)Q[r], 0xAA, 0xf, 0xf, true);
            qf[3][r] = (unsigned)__builtin_amdgcn_mov_dpp((int)Q[r], 0xFF, 0xf, 0xf, true);
        }
    }
    float m2;
    {
        f32x4 S[8];
        m2 = qk_all(S, K, qf);
        v4u P = make_p(S[0], S[1], m2);
        SCHED();
#pragma unroll
        for (int u = 0; u < 4; ++u) {
            asm volatile("s_nop 1" : "+v"(P));
            const v4u Pu = P;
            float e[8];
            if (u == 0) pv_step<true>(acc[8], Pu, ones4); else pv_step<false>(acc[8], Pu, ones4);
            SCHED();
#pragma unroll
            for (int jj = 0; jj < 8; ++jj) {
                if (u < 3) e[jj] = ex2(fmaf(S[2 * u + 2 + (jj >> 2)][jj & 3], SCL, -m2));
                if (u < 3 && (jj & 1)) P[jj >> 1] = cvtpk(e[jj - 1], e[jj]);
                SCHED();
                if (u == 0) pv_step<true>(acc[jj], Pu, Vt[u][jj]); else pv_step<false>(acc[jj], Pu, Vt[u][jj]);
                SCHED();
            }
        }
    }
    merge_publish(x, acc, m2, MA1);
    wo_phase<false>(x, 1, Tag, [&]() {});
    v4u ER[4] = {{0, 0, 0, 0}, {0, 0, 0, 0}, {0, 0, 0, 0}, {0, 0, 0, 0}};
    if (x.c == 0 && w == 3) {
        const int le = fresh_lane();
        asm volatile("buffer_load_dwordx4 %0, %4, %5, 0 offen sc1\n\tbuffer_load_dwordx4 %1, %4, %5, 0 offen offset:1024 sc1\n\t"
                     "buffer_load_dwordx4 %2, %4, %5, 0 offen offset:2048 sc1\n\tbuffer_load_dwordx4 %3, %4, %5, 0 offen offset:3072 sc1"
                     : "=&v"(ER[0]), "=&v"(ER[1]), "=&v"(ER[2]), "=&v"(ER[3]) : "v"(OFF_READY + le * 16), "s"(x.ws) : "memory");
    }
    {
        const int l3 = fresh_lane(), I = 8 * x.g + x.s;
        GAS char* op = (GAS char*)(out + x.b * 512 + 16 * (I >> 1) + 2 * (I & 1) + 2 * l3);
        asm volatile("" : "+v"(op));
        const float r = mlp_phase(x, 1, x2v, CF, b2, [&](int) {});
        if (w == 0) {
            const unsigned pk = pack2(r);
            if ((l3 & ~6) == 0) *(GAS unsigned*)op = pk;
        }
        if (x.c == 0 && w == 3) {
            asm volatile("" : "+v"(ER[0]), "+v"(ER[1]), "+v"(ER[2]), "+v"(ER[3]));
            unsigned bad = 0;
#pragma unroll
            for (int m = 0; m < 4; ++m) bad |= (ER[m].x ^ Tag) | (ER[m].y ^ Tag) | (ER[m].z ^ Tag) | (ER[m].w ^ Tag);
            for (unsigned spin = 0; __builtin_amdgcn_ballot_w64(bad != 0u) != 0ull && spin < SPIN_MAX; ++spin) {
                MEMFENCE();
                bad = 0;
#pragma unroll
                for (int m = 0; m < 4; ++m) { const v4u rd = ld128(x.ws, OFF_READY + (m * 64 + l3) * 16); bad |= (rd.x ^ Tag) | (rd.y ^ Tag) | (rd.z ^ Tag) | (rd.w ^ Tag); }
            }
            if (__builtin_amdgcn_ballot_w64(bad != 0u) == 0ull && l3 == 0) st32(x.ws, OFF_E, Tag);
        }
    }
}

}

extern "C" void kernel_launch(void* const* d_in, const int* in_sizes, int n_in, void* d_out, int out_size, void* d_ws, size_t ws_size, hipStream_t stream) {
    (void)in_sizes; (void)n_in; (void)out_size; (void)ws_size;
    (void)hipFuncSetAttribute((const void*)mega_kernel, hipFuncAttributeMaxDynamicSharedMemorySize, LDS_BYTES);
    mega_kernel<<<256, 256, LDS_BYTES, stream>>>((const u16*)d_in[0], (const u16*)d_in[1], (const u16*)d_in[2], (const u16*)d_in[3], (const u16*)d_in[4],
                                                 (const u16*)d_in[6], (const u16*)d_in[7], (const u16*)d_in[8], (const u16*)d_in[9], (u16*)d_out, (char*)d_ws);
}
```
